```python
import torch

def _softmax(x, axis=-1):
    return torch.softmax(x, dim=axis)
def _gelu(x):
    return torch.nn.functional.gelu(x, approximate="tanh")
def _silu(x):
    return torch.nn.functional.silu(x)
def _sigmoid(x):
    return torch.sigmoid(x)
def _layernorm(x, axis=-1, eps=1e-5):
    m = x.mean(dim=axis, keepdim=True)
    v = x.var(dim=axis, keepdim=True, unbiased=False)
    return (x - m) / torch.sqrt(v + eps)
def _scatter_add(tgt, idx, src, axis=0):
    return tgt.clone().index_add_(axis, idx, src)
def _segment_sum(src, seg, n):
    out = torch.zeros((n,) + src.shape[1:], dtype=src.dtype)
    return out.index_add_(0, seg, src)
def _pad(x, pw):
    flat = tuple(v for pair in reversed(pw) for v in pair)
    return torch.nn.functional.pad(x, flat)

kernel_name = 'gated_mix__sliding_block_cached__dense_activation_gelu__decode'

def setup_inputs():

    _w = (lambda a: (lambda t: t.to(torch.bfloat16) if t.is_floating_point() else t)(torch.as_tensor(a)))
    def _A_setup():
        B, Sq, D = 1, 1, 512
        Hq, Hkv, Sk, Dh = 32, 8, 65536, 128
        assert Hq % Hkv == 0, (Hq, Hkv)
        return {
            "x": _w((lambda *s: torch.randn(*s, dtype=torch.bfloat16) * 0.05)(B, Sq, D)),
            "k_cache": _w((lambda *s: torch.randn(*s, dtype=torch.bfloat16) * 3.2)(B, Hkv, Sk, Dh)),
            "v_cache": _w((lambda *s: torch.randn(*s, dtype=torch.bfloat16) * 0.4)(B, Hkv, Sk, Dh)),
            "wq": _w((lambda *s: torch.randn(*s, dtype=torch.bfloat16) * 3.2)(D, Hq * Dh) * (D ** -0.5)),
            "wo": _w((lambda *s: torch.randn(*s, dtype=torch.bfloat16) * 0.4)(Hq * Dh, D) * ((Hq * Dh) ** -0.5)),
        }
    _A_in = _A_setup()

    def _B_setup():
        B, S, D, F = 1, 1, 512, 1792
        return {
            "x": _w((lambda *s: torch.randn(*s, dtype=torch.bfloat16) * 0.05)(B, S, D)),
            "w1": _w((lambda *s: torch.randn(*s, dtype=torch.bfloat16) * 0.2)(D, F) * (D ** -0.5)),
            "b1": _w((lambda *s: torch.randn(*s, dtype=torch.bfloat16) * 0.2)(F) * 0.01),
            "w2": _w((lambda *s: torch.randn(*s, dtype=torch.bfloat16) * 0.2)(F, D) * (F ** -0.5)),
            "b2": _w((lambda *s: torch.randn(*s, dtype=torch.bfloat16) * 0.05)(D) * 0.01),
        }
    _B_in = _B_setup()

    return {
        "A_x": _A_in["x"],
        "A_k_cache": _A_in["k_cache"],
        "A_v_cache": _A_in["v_cache"],
        "A_wq": _A_in["wq"],
        "A_wo": _A_in["wo"],
        "B_x": _B_in["x"],
        "B_w1": _B_in["w1"],
        "B_b1": _B_in["b1"],
        "B_w2": _B_in["w2"],
        "B_b2": _B_in["b2"],
        "gate_w": _w((lambda *s: torch.randn(*s, dtype=torch.bfloat16) * 0.1)(512) * 16.0),
    }

def reference(A_x, A_k_cache, A_v_cache, A_wq, A_wo, B_x, B_w1, B_b1, B_w2, B_b2, gate_w):
    def _A_ref(x, k_cache, v_cache, wq, wo):
        B, Sq, D = x.shape
        Hq, Dh = 32, 128
        Hkv, Sk = k_cache.shape[1], k_cache.shape[2]
        W = min(4096, max(256, Sk // 4))
        rep = Hq // Hkv
        q = (lambda x, *d: x.permute(*d))((x @ wq).reshape(B, Sq, Hq, Dh), 0, 2, 1, 3)
        k = (lambda x, n, axis: torch.repeat_interleave(x, n, dim=axis))(k_cache[:, :, Sk - W:, :], rep, axis=1)
        v = (lambda x, n, axis: torch.repeat_interleave(x, n, dim=axis))(v_cache[:, :, Sk - W:, :], rep, axis=1)
        scores = torch.einsum("bhqd,bhkd->bhqk", q, k) * (Dh ** -0.5)
        p = _softmax(scores, axis=-1)
        o = torch.einsum("bhqk,bhkd->bhqd", p, v)
        return (lambda x, *d: x.permute(*d))(o, 0, 2, 1, 3).reshape(B, Sq, Hq * Dh) @ wo

    def _B_ref(x, w1, b1, w2, b2):
        h = torch.einsum("bsd,df->bsf", x, w1) + b1
        h = _gelu(h)
        return torch.einsum("bsf,fd->bsd", h, w2) + b2

    x = A_x
    g = _sigmoid((x * gate_w).sum(-1, keepdims=True))
    a = _A_ref(x, A_k_cache, A_v_cache, A_wq, A_wo)
    b = _B_ref(x, B_w1, B_b1, B_w2, B_b2)
    return g * a + (1 - g) * b
```

```cpp
#include <hip/hip_runtime.h>
#include <hip/hip_bf16.h>
#include <stdint.h>

typedef unsigned short u16;
typedef unsigned u32x4 __attribute__((ext_vector_type(4)));
__device__ __forceinline__ float bf2f(u16 b) { return __builtin_bit_cast(float, (unsigned)b << 16); }
__device__ __forceinline__ float lo(unsigned w) { return __builtin_bit_cast(float, w << 16); }
__device__ __forceinline__ float hi(unsigned w) { return __builtin_bit_cast(float, w & 0xffff0000u); }
__device__ __forceinline__ float rbf(float f) { return __bfloat162float(__float2bfloat16(f)); }

constexpr int D = 512, HQ = 32, HKV = 8, SK = 65536, DH = 128, W = 4096, F = 1792;
constexpr int NSPLIT = 32, KPS = W / NSPLIT;

__global__ void __launch_bounds__(256) k1(const u16* x, const u16* wq, const u16* w1, const u16* b1, float* q, float* h) {
  __shared__ float red[32][65];
  const int b = blockIdx.x, t = threadIdx.x;
  const bool isq = b < 64;
  const u16* Wm = isq ? wq + b * 64 : w1 + (b - 64) * 64;
  const int ld = isq ? 4096 : F;
  const int cg = t & 7, rg = t >> 3;
  float acc[8] = {0};
  for (int r = rg; r < D; r += 32) {
    u32x4 w = *(const u32x4*)(Wm + (size_t)r * ld + cg * 8);
    float xv = bf2f(x[r]);
    for (int j = 0; j < 4; ++j) { acc[2 * j] += xv * lo(w[j]); acc[2 * j + 1] += xv * hi(w[j]); }
  }
  for (int j = 0; j < 8; ++j) red[rg][cg * 8 + j] = acc[j];
  __syncthreads();
  if (t < 64) {
    float s = 0;
    for (int r = 0; r < 32; ++r) s += red[r][t];
    if (isq) q[b * 64 + t] = rbf(s);
    else {
      int c = (b - 64) * 64 + t;
      float v = rbf(rbf(s) + bf2f(b1[c]));
      float u = 0.7978845608028654f * (v + 0.044715f * v * v * v);
      float g = 0.5f * v * (1.f + tanhf(u));
      h[c] = rbf(g);
    }
  }
}

__global__ void __launch_bounds__(256) k2(const float* q, const u16* kc, const u16* vc, float* po, float* pml) {
  __shared__ float sq[4][128];
  __shared__ float sp[4][KPS];
  __shared__ float sacc[16][4][128];
  __shared__ float sm[4], sl[4];
  const int kvh = blockIdx.x & 7, s = blockIdx.x >> 3, t = threadIdx.x;
  for (int i = t; i < 512; i += 256) sq[i >> 7][i & 127] = q[kvh * 512 + i];
  __syncthreads();
  const u16* kb = kc + ((size_t)kvh * SK + (SK - W) + s * KPS) * DH;
  const u16* vb = vc + ((size_t)kvh * SK + (SK - W) + s * KPS) * DH;
  const int c = t & 15, r = t >> 4;
  for (int k = r; k < KPS; k += 16) {
    u32x4 w = *(const u32x4*)(kb + k * DH + c * 8);
    float d[4];
    for (int g = 0; g < 4; ++g) {
      float a = 0;
      for (int j = 0; j < 4; ++j) a += lo(w[j]) * sq[g][c * 8 + 2 * j] + hi(w[j]) * sq[g][c * 8 + 2 * j + 1];
      for (int m = 8; m >= 1; m >>= 1) a += __shfl_xor(a, m);
      d[g] = a;
    }
    if (c < 4) sp[c][k] = d[c] * 0.08838834764831845f;
  }
  __syncthreads();
  {
    const int g = t >> 6, l = t & 63;
    float a = sp[g][l], b = sp[g][l + 64];
    float m = fmaxf(a, b);
    for (int o = 32; o >= 1; o >>= 1) m = fmaxf(m, __shfl_xor(m, o));
    a = __expf(a - m); b = __expf(b - m);
    float su = a + b;
    for (int o = 32; o >= 1; o >>= 1) su += __shfl_xor(su, o);
    sp[g][l] = a; sp[g][l + 64] = b;
    if (l == 0) { sm[g] = m; sl[g] = su; }
  }
  __syncthreads();
  float acc[4][8] = {{0}};
  for (int k = r; k < KPS; k += 16) {
    u32x4 w = *(const u32x4*)(vb + k * DH + c * 8);
    for (int g = 0; g < 4; ++g) { float p = sp[g][k];
      for (int j = 0; j < 4; ++j) { acc[g][2 * j] += p * lo(w[j]); acc[g][2 * j + 1] += p * hi(w[j]); } }
  }
  for (int g = 0; g < 4; ++g) for (int j = 0; j < 8; ++j) sacc[r][g][c * 8 + j] = acc[g][j];
  __syncthreads();
  for (int i = t; i < 512; i += 256) {
    float a = 0;
    for (int rr = 0; rr < 16; ++rr) a += sacc[rr][i >> 7][i & 127];
    po[((size_t)(kvh * 4 + (i >> 7)) * NSPLIT + s) * 128 + (i & 127)] = a;
  }
  if (t < 4) { pml[((kvh * 4 + t) * NSPLIT + s) * 2] = sm[t]; pml[((kvh * 4 + t) * NSPLIT + s) * 2 + 1] = sl[t]; }
}

__global__ void __launch_bounds__(256) k3(const float* po, const float* pml, const float* h, const u16* wo, const u16* w2, float* part) {
  __shared__ float so[32];
  __shared__ float red[4][512];
  const int b = blockIdx.x, t = threadIdx.x;
  const u16* Wm;
  if (b < 128) {
    const int hd = b >> 2, d0 = (b & 3) * 32;
    if (t < 32) {
      float M = -INFINITY;
      for (int s = 0; s < NSPLIT; ++s) M = fmaxf(M, pml[(hd * NSPLIT + s) * 2]);
      float L = 0, o = 0;
      for (int s = 0; s < NSPLIT; ++s) { float w = __expf(pml[(hd * NSPLIT + s) * 2] - M); L += w * pml[(hd * NSPLIT + s) * 2 + 1]; o += w * po[((size_t)hd * NSPLIT + s) * 128 + d0 + t]; }
      so[t] = rbf(o / L);
    }
    Wm = wo + (size_t)b * 32 * D;
  } else {
    if (t < 32) so[t] = h[(b - 128) * 32 + t];
    Wm = w2 + (size_t)(b - 128) * 32 * D;
  }
  __syncthreads();
  const int l = t & 63, w = t >> 6;
  float acc[8] = {0};
  for (int r = w; r < 32; r += 4) {
    u32x4 v = *(const u32x4*)(Wm + (size_t)r * D + l * 8);
    float xv = so[r];
    for (int j = 0; j < 4; ++j) { acc[2 * j] += xv * lo(v[j]); acc[2 * j + 1] += xv * hi(v[j]); }
  }
  for (int j = 0; j < 8; ++j) red[w][l * 8 + j] = acc[j];
  __syncthreads();
  for (int i = t; i < 512; i += 256) part[b * 512 + i] = red[0][i] + red[1][i] + red[2][i] + red[3][i];
}

__global__ void __launch_bounds__(256) k4(const float* part, const u16* x, const u16* gw, const u16* b2, __hip_bfloat16* out) {
  __shared__ float ra[4][64], rb[4][64], sg[4];
  const int t = threadIdx.x, l = t & 63, w = t >> 6, c = blockIdx.x * 64 + l;
  float a = 0, b = 0;
  for (int i = w; i < 128; i += 4) a += part[i * 512 + c];
  for (int i = 128 + w; i < 184; i += 4) b += part[i * 512 + c];
  ra[w][l] = a; rb[w][l] = b;
  float gs = rbf(bf2f(x[t]) * bf2f(gw[t])) + rbf(bf2f(x[t + 256]) * bf2f(gw[t + 256]));
  for (int o = 32; o >= 1; o >>= 1) gs += __shfl_xor(gs, o);
  if (l == 0) sg[w] = gs;
  __syncthreads();
  if (t < 64) {
    float z = rbf(sg[0] + sg[1] + sg[2] + sg[3]);
    float g = rbf(1.f / (1.f + __expf(-z)));
    float A = rbf(ra[0][l] + ra[1][l] + ra[2][l] + ra[3][l]);
    float B = rbf(rbf(rb[0][l] + rb[1][l] + rb[2][l] + rb[3][l]) + bf2f(b2[c]));
    out[c] = __float2bfloat16(rbf(g * A) + rbf(rbf(1.f - g) * B));
  }
}

extern "C" void kernel_launch(void* const* d_in, const int* in_sizes, int n_in, void* d_out, int out_size, void* d_ws, size_t ws_size, hipStream_t stream) {
  const u16* x = (const u16*)d_in[0]; const u16* kc = (const u16*)d_in[1]; const u16* vc = (const u16*)d_in[2];
  const u16* wq = (const u16*)d_in[3]; const u16* wo = (const u16*)d_in[4];
  const u16* w1 = (const u16*)d_in[6]; const u16* b1 = (const u16*)d_in[7]; const u16* w2 = (const u16*)d_in[8]; const u16* b2 = (const u16*)d_in[9];
  const u16* gw = (const u16*)d_in[10];
  float* ws = (float*)d_ws;
  float* q = ws; float* h = ws + 4096; float* pml = ws + 8192; float* po = ws + 16384; float* part = po + 32 * 32 * 128;
  k1<<<64 + 28, 256, 0, stream>>>(x, wq, w1, b1, q, h);
  k2<<<256, 256, 0, stream>>>(q, kc, vc, po, pml);
  k3<<<184, 256, 0, stream>>>(po, pml, h, wo, w2, part);
  k4<<<8, 256, 0, stream>>>(part, x, gw, b2, (__hip_bfloat16*)d_out);
}
```

```cpp
#include <hip/hip_runtime.h>
#include <hip/hip_bf16.h>
#include <stdint.h>

#define WIN 24
#define QDELAY 0
#define ODELAY 0
#define FDELAY 0
#define STAMPS 0

typedef unsigned short u16;
typedef unsigned long long u64;
typedef unsigned u32x4 __attribute__((ext_vector_type(4)));
typedef unsigned u32x2 __attribute__((ext_vector_type(2)));
typedef float f32x4 __attribute__((ext_vector_type(4)));
typedef float f32x2 __attribute__((ext_vector_type(2)));
typedef short s16x4 __attribute__((ext_vector_type(4)));
typedef short s16x8 __attribute__((ext_vector_type(8)));
typedef __bf16 bf16x8 __attribute__((ext_vector_type(8)));
typedef __bf16 bf16x2 __attribute__((ext_vector_type(2)));
#define GAS __attribute__((address_space(1)))
#define LAS __attribute__((address_space(3)))
#define CAS __attribute__((address_space(4)))

constexpr unsigned L_WQ = 0, L_K = 16384, L_V = 49152, L_W2 = 81920, L_WO = 89088, L_W1 = 105472, L_X = 112640, L_Q = 112896  ,
                   L_S = 114176  , L_OP = 116288  , L_H = 118336  , L_FLG = 118400  , L_SCR = 118464  , L_END = 119488;
constexpr unsigned SSTR = 528, QSTR = 320;
constexpr unsigned W_E = 0, W_QG = 4096, W_O = W_QG + 131072, W_ML = W_O + 524288, W_H = W_ML + 8192, W_P3 = W_H + 262144, W_END = W_P3 + 262144, W_STAMP = 2u << 20;
constexpr unsigned SENT = 0xFFFFFFFFu;
constexpr int SPIN_MAX = 1 << 17;
constexpr int AUX_SC1 = 16;

template <int N> __device__ __forceinline__ void waitvm() { asm volatile("s_waitcnt vmcnt(%0)" ::"i"(N) : "memory"); }
__device__ __forceinline__ void waitlgkm() { asm volatile("s_waitcnt lgkmcnt(0)" ::: "memory"); }
__device__ __forceinline__ void bar() { asm volatile("s_waitcnt lgkmcnt(0)\n\ts_barrier" ::: "memory"); }
__device__ __forceinline__ void dma1(const void* sbase, unsigned v0, unsigned m0v) {
  asm volatile("s_mov_b32 m0, %2\n\ts_nop 0\n\tglobal_load_lds_dwordx4 %0, %1 nt" ::"v"(v0), "s"(sbase), "s"(m0v) : "memory");
}
__device__ __forceinline__ void dma2(const void* sbase, unsigned v0, unsigned v1, unsigned m0v) {
  asm volatile("s_mov_b32 m0, %3\n\ts_nop 0\n\tglobal_load_lds_dwordx4 %0, %2 nt\n\tglobal_load_lds_dwordx4 %1, %2 offset:1024 nt" ::"v"(v0), "v"(v1), "s"(sbase), "s"(m0v) : "memory");
}
__device__ __forceinline__ void dma4(const void* sbase, unsigned v0, unsigned v1, unsigned v2, unsigned v3, unsigned m0v) {
  asm volatile("s_mov_b32 m0, %5\n\ts_nop 0\n\tglobal_load_lds_dwordx4 %0, %4 nt\n\tglobal_load_lds_dwordx4 %1, %4 offset:1024 nt\n\t"
               "global_load_lds_dwordx4 %2, %4 offset:2048 nt\n\tglobal_load_lds_dwordx4 %3, %4 offset:3072 nt" ::"v"(v0), "v"(v1), "v"(v2), "v"(v3), "s"(sbase), "s"(m0v) : "memory");
}
template <int CTRL> __device__ __forceinline__ float dpp(float x) { return __builtin_bit_cast(float, __builtin_amdgcn_mov_dpp(__builtin_bit_cast(int, x), CTRL, 0xf, 0xf, true)); }
template <int CTRL> __device__ __forceinline__ unsigned dppu(unsigned x) { return (unsigned)__builtin_amdgcn_mov_dpp((int)x, CTRL, 0xf, 0xf, true); }
constexpr int XOR1 = 0xB1, XOR2 = 0x4E, HMIR = 0x141, ROR4 = 0x124, ROR8 = 0x128, ROR2 = 0x122, ROR1 = 0x121, BC0 = 0x150;
__device__ __forceinline__ float sw32add(float a, float b) {
  auto s = __builtin_amdgcn_permlane32_swap(__float_as_uint(a), __float_as_uint(b), false, false);
  return __uint_as_float(s[0]) + __uint_as_float(s[1]);
}
__device__ __forceinline__ float sw16add(float a, float b) {
  auto s = __builtin_amdgcn_permlane16_swap(__float_as_uint(a), __float_as_uint(b), false, false);
  return __uint_as_float(s[0]) + __uint_as_float(s[1]);
}
__device__ __forceinline__ float rowsmax(float x) {
  auto s = __builtin_amdgcn_permlane16_swap(__float_as_uint(x), __float_as_uint(x), false, false);
  x = fmaxf(__uint_as_float(s[0]), __uint_as_float(s[1]));
  auto t = __builtin_amdgcn_permlane32_swap(__float_as_uint(x), __float_as_uint(x), false, false);
  return fmaxf(__uint_as_float(t[0]), __uint_as_float(t[1]));
}
__device__ __forceinline__ float rowssum(float x) { x = sw16add(x, x); return sw32add(x, x); }
__device__ __forceinline__ float tr4(float v0, float v1, float v2, float v3) {
  return sw16add(sw32add(v0, v2), sw32add(v1, v3));
}
__device__ __forceinline__ unsigned pk(float lo, float hi) { return __builtin_bit_cast(unsigned, __builtin_convertvector(f32x2{lo, hi}, bf16x2)); }
__device__ __forceinline__ float bflo(unsigned w) { return __uint_as_float(w << 16); }
__device__ __forceinline__ float bfhi(unsigned w) { return __uint_as_float(w & 0xffff0000u); }
__device__ __forceinline__ float rbf(float x) { return bflo(pk(x, x)); }
__device__ __forceinline__ f32x4 mfma32(u32x4 a, u32x4 b, f32x4 c) { return __builtin_amdgcn_mfma_f32_16x16x32_bf16(__builtin_bit_cast(bf16x8, a), __builtin_bit_cast(bf16x8, b), c, 0, 0, 0); }
__device__ __forceinline__ u32x2 trd(LAS char* p) { return __builtin_bit_cast(u32x2, __builtin_amdgcn_ds_read_tr16_b64_v4i16((LAS s16x4*)p)); }
__device__ __forceinline__ u32x4 trd2(LAS char* p0, LAS char* p1) { u32x2 a = trd(p0), b = trd(p1); return u32x4{a.x, a.y, b.x, b.y}; }
__device__ __forceinline__ unsigned clean(float v) { return min(__float_as_uint(v), 0xFFFFFFFEu); }
__device__ __forceinline__ void lds_wait_ge(LAS char* p, unsigned n) {
#pragma unroll 1
  for (int i = 0; i < (1 << 22); ++i) {
    if (__builtin_amdgcn_readfirstlane(*(volatile LAS unsigned*)p) >= n) break;
    __builtin_amdgcn_s_sleep(1);
  }
  asm volatile("" ::: "memory");
}
__device__ __forceinline__ void lds_arrive(LAS char* p, int l) {
  waitlgkm();
  if (l == 0) __hip_atomic_fetch_add((LAS unsigned*)p, 1u, __ATOMIC_RELAXED, __HIP_MEMORY_SCOPE_WORKGROUP);
}
#define STAMP(i)

__global__ void __launch_bounds__(640) fused_kernel(const u16* __restrict__ x, const u16* __restrict__ kc, const u16* __restrict__ vc, const u16* __restrict__ wq,
                                                    const u16* __restrict__ wo, const u16* __restrict__ w1, const u16* __restrict__ b1, const u16* __restrict__ w2,
                                                    const u16* __restrict__ b2, const u16* __restrict__ gw, u16* __restrict__ out, char* __restrict__ ws) {
  __shared__ __attribute__((aligned(1024))) char smem[L_END];
  LAS char* L = (LAS char*)smem;
  const unsigned lbase = (unsigned)(size_t)L;
  const int b = blockIdx.x, wv = __builtin_amdgcn_readfirstlane(threadIdx.x >> 6), l = threadIdx.x & 63;
  const int g = b & 7, j = b >> 3, g4 = l >> 4, c = l & 15;
  const int rbl = j >> 2, cb = j & 3, rbg = g * 8 + rbl;
  const __amdgpu_buffer_rsrc_t rs = __builtin_amdgcn_make_buffer_rsrc(ws, 0, W_END, 0x00020000);

  if (wv == 8) {
    const unsigned bc = (unsigned)(c ^ (g4 << 2));
    unsigned vk[4], vw[4];
#pragma unroll
    for (int k = 0; k < 4; ++k) { vk[k] = g4 * 256 + ((bc ^ k) << 4); vw[k] = k * 3072 + g4 * 1024 + ((bc ^ k) << 4); }
    const size_t kvoff = ((size_t)g * 65536 + 61440 + j * 128) * 256;
    const char* kb = (const char*)kc + kvoff;
    const char* vb = (const char*)vc + kvoff;
    const char* w2b = (const char*)w2 + (size_t)rbg * 28 * 1024 + cb * 256;
    const char* wob = (const char*)wo + (size_t)rbg * 64 * 1024 + cb * 256;
#define GRP(base, v, m0v) dma4(base, v[0], v[1], v[2], v[3], lbase + (m0v))
    GRP(kb, vk, L_K);
    GRP(kb + 4096, vk, L_K + 4096);
    STAMP(1);
    bar();
    waitvm<4>();
    GRP(kb + 2 * 4096, vk, L_K + 2 * 4096);
    GRP(kb + 3 * 4096, vk, L_K + 3 * 4096);
    GRP(kb + 4 * 4096, vk, L_K + 4 * 4096);
    waitvm<12>();
    GRP(kb + 5 * 4096, vk, L_K + 5 * 4096);
    GRP(kb + 6 * 4096, vk, L_K + 6 * 4096);
    waitvm<WIN - 4>();
    GRP(kb + 7 * 4096, vk, L_K + 7 * 4096);
    constexpr int NKF = WIN / 4 - 1;
#pragma unroll
    for (int n = 0; n < 8; ++n) {
      waitvm<WIN - 4>();
      if (n == NKF) { *(volatile LAS unsigned*)(L + L_FLG + 16) = 1u; STAMP(2); }
      GRP(vb + n * 4096, vk, L_V + n * 4096);
    }
    waitvm<WIN - 4>();
    GRP(w2b, vw, L_W2);
    waitvm<WIN - 4>();
    dma2(w2b + 16384, vw[0], vw[1], lbase + L_W2 + 4096); dma1(w2b + 16384, vw[2] + 2048, lbase + L_W2 + 4096 + 2048);
#pragma unroll
    for (int n = 0; n < 4; ++n) {
      if (WIN - 4 >= 7 + 4 * n) waitvm<WIN - 4>();
      GRP(wob + n * 16384, vw, L_WO + n * 4096);
    }
    STAMP(3);
    waitvm<23>();
    STAMP(4);
    bar();
    waitvm<0>();
    STAMP(5);
    bar();
  } else if (wv == 9) {
    const u32x4 s4 = {SENT, SENT, SENT, SENT};
    __builtin_amdgcn_raw_buffer_store_b128(s4, rs, W_O + ((g * 8 + g4) * 32 + j) * 256 + c * 16, 0, AUX_SC1);
    __builtin_amdgcn_raw_buffer_store_b128(s4, rs, W_O + ((g * 8 + 4 + g4) * 32 + j) * 256 + c * 16, 0, AUX_SC1);
    __builtin_amdgcn_raw_buffer_store_b128(s4, rs, W_H + ((g * 8 + (l >> 3)) * 32 + j) * 128 + (l & 7) * 16, 0, AUX_SC1);
    if (l < 4) __builtin_amdgcn_raw_buffer_store_b64(u32x2{SENT, SENT}, rs, W_ML + ((g * 4 + l) * 32 + j) * 8, 0, AUX_SC1);
    asm volatile("" ::: "memory");
    const u32x4 xg = *(const GAS u32x4*)(x + 8 * l), gg = *(const GAS u32x4*)(gw + 8 * l);
    const u32x2 xf = *(const GAS u32x2*)(x + 16 * j + 4 * g4);
    const int unit = 4 * (c & 7) + g4;
    const unsigned b1v = *(const GAS u16*)(b1 + rbg * 28 + (unit < 28 ? unit : 0));
    asm volatile("" ::: "memory");
    {
      const char* w1b = (const char*)w1 + (size_t)j * 16 * 3584 + g * 448;
      int row = l / 28, ch = l - row * 28;
#pragma unroll
      for (int p = 0; p < 7; ++p) {
        dma1(w1b, row * 3584 + ch * 16, lbase + L_W1 + p * 1024);
        ch += 8; row += 2;
        if (ch >= 28) { ch -= 28; row += 1; }
      }
    }
    bar();
    waitvm<7>();
    *(volatile LAS unsigned*)(L + L_FLG + 20) = 1u;
    STAMP(1);
    {
      float z = 0.f;
#pragma unroll
      for (int k = 0; k < 4; ++k) z += rbf(bflo(xg[k]) * bflo(gg[k])) + rbf(bfhi(xg[k]) * bfhi(gg[k]));
      z += dpp<ROR8>(z); z += dpp<ROR4>(z); z += dpp<ROR2>(z); z += dpp<ROR1>(z);
      z = rbf(rowssum(z));
      *(volatile LAS float*)(L + L_FLG) = rbf(1.f / (1.f + __expf(-z)));
    }
    waitvm<0>();
    STAMP(2);
    f32x4 acc = {0.f, 0.f, 0.f, 0.f};
    LAS char* wp = L + L_W1 + (4 * g4 + (c >> 2)) * 448 + 8 * (c & 3);
#pragma unroll
    for (int t = 0; t < 14; ++t) {
      const u32x2 a = trd(wp + 32 * t);
      const u32x2 bb = {c == t ? xf.x : 0u, c == t ? xf.y : 0u};
      acc = __builtin_amdgcn_mfma_f32_16x16x16bf16_1k(__builtin_bit_cast(s16x4, a), __builtin_bit_cast(s16x4, bb), acc, 0, 0, 0);
    }
    {
      const int u4 = 4 * c + g4, r7 = u4 / 7, pos = u4 - 7 * r7;
      if (c < 14) __builtin_amdgcn_raw_buffer_store_b128(u32x4{clean(acc[0]), clean(acc[1]), clean(acc[2]), clean(acc[3])}, rs, W_H + ((g * 8 + r7) * 32 + j) * 128 + pos * 16, 0, AUX_SC1);
    }
    STAMP(3);
    lds_wait_ge(L + L_FLG + 4, 8);
    STAMP(4);
    f32x4 hs;
    _Pragma("unroll 1") for (int sp = 0; sp < SPIN_MAX; ++sp) {
      asm volatile("" ::: "memory");
      u32x4 v[4];
#pragma unroll
      for (int i = 0; i < 4; ++i) v[i] = __builtin_amdgcn_raw_buffer_load_b128(rs, W_H + rbg * 4096 + i * 1024 + l * 16, 0, AUX_SC1);
      unsigned mx = 0;
#pragma unroll
      for (int i = 0; i < 4; ++i) mx = max(max(max(v[i].x, v[i].y), max(v[i].z, v[i].w)), mx);
      hs = (__builtin_bit_cast(f32x4, v[0]) + __builtin_bit_cast(f32x4, v[1])) + (__builtin_bit_cast(f32x4, v[2]) + __builtin_bit_cast(f32x4, v[3]));
      if (__all(mx != SENT || (l & 7) == 7)) break;
    }
    float h = tr4(hs[0], hs[1], hs[2], hs[3]);
    h += dpp<ROR8>(h);
    h = rbf(rbf(h) + bflo(b1v));
    const float u2 = h * (1.5957691216057308f + 0.07135481282466138f * h * h);
    h = h / (1.f + __expf(-u2));
    if (c < 8) *(volatile LAS u16*)(L + L_H + unit * 2) = unit < 28 ? (u16)pk(h, h) : (u16)0;
    waitlgkm();
    STAMP(5);
  } else {
    const u64 E = *(const CAS u64*)(ws + W_E) + 1;
    const unsigned Tlo = (unsigned)E, Thi = (unsigned)(E >> 8);
    if (wv == 0) {
      __builtin_amdgcn_s_setprio(3);
      const int rq = j & 3;
      asm volatile("s_mov_b32 m0, %2\n\ts_nop 0\n\tglobal_load_lds_dword %0, %1" ::"v"(l * 4), "s"(x + 128 * rq), "s"(lbase + L_X) : "memory");
      const char* wqb = (const char*)wq + (size_t)rq * 128 * 8192 + g * 1024 + (j >> 2) * 128;
      const unsigned sc = (unsigned)((l & 7) ^ ((g4 & 1) << 1));
      const unsigned v0 = (l >> 3) * 8192 + (sc << 4), v1 = (l >> 3) * 8192 + ((sc ^ 4) << 4) + 65536 - 1024;
#pragma unroll
      for (int p = 0; p < 8; ++p) dma2(wqb + p * 131072, v0, v1, lbase + L_WQ + p * 2048);
      STAMP(1);
      bar();
      f32x4 a4[4];
#pragma unroll
      for (int t = 0; t < 4; ++t) a4[t] = f32x4{0.f, 0.f, 0.f, 0.f};
      const int q = c >> 2, p = c & 3;
      LAS char* wp = L + L_WQ + (8 * g4 + q) * 128 + 8 * (p & 1);
      const int hx = ((q >> 1) | ((g4 & 1) << 1)) << 1;
#pragma unroll
      for (int ks = 0; ks < 4; ++ks) {
        if (ks == 0) waitvm<12>(); else if (ks == 1) waitvm<8>(); else if (ks == 2) waitvm<4>(); else waitvm<0>();
        const u32x4 xa = *(LAS u32x4*)(L + L_X + ks * 64 + g4 * 16);
#pragma unroll
        for (int t = 0; t < 4; ++t) {
          LAS char* a = wp + ks * 4096 + (((2 * t + (p >> 1)) ^ hx) << 4);
          a4[t] = mfma32(xa, trd2(a, a + 512), a4[t]);
        }
      }
      const float qv = g4 == 0 ? a4[0][0] : g4 == 1 ? a4[1][0] : g4 == 2 ? a4[2][0] : a4[3][0];
      lds_wait_ge(L + L_FLG + 20, 1);
      __builtin_amdgcn_raw_buffer_store_b64(u32x2{__builtin_amdgcn_alignbit(Tlo, __float_as_uint(qv), 8), Thi}, rs, W_QG + ((g * 4 + rq) * 512 + (j >> 2) * 64 + l) * 8, 0, AUX_SC1);
      __builtin_amdgcn_s_setprio(0);
      STAMP(2);
    } else {
      if (wv == 1 && l < 8) *(volatile LAS unsigned*)(L + L_FLG + 4 * l) = 0u;
      bar();
      lds_wait_ge(L + L_FLG + 20, 1);
    }
    if (QDELAY) __builtin_amdgcn_s_sleep(QDELAY);
    {
      float q0, q1;
      _Pragma("unroll 1") for (int sp = 0; sp < SPIN_MAX; ++sp) {
        asm volatile("" ::: "memory");
        const unsigned o = W_QG + ((g * 4 + (l >> 5)) * 512 + wv * 64 + 2 * (l & 31)) * 8;
        const u32x4 a = __builtin_amdgcn_raw_buffer_load_b128(rs, o, 0, AUX_SC1), d = __builtin_amdgcn_raw_buffer_load_b128(rs, o + 8192, 0, AUX_SC1);
        const unsigned T24 = Tlo << 24;
        const unsigned bad = (((a.x ^ T24) | (a.z ^ T24) | (d.x ^ T24) | (d.z ^ T24)) >> 24) | (a.y ^ Thi) | (a.w ^ Thi) | (d.y ^ Thi) | (d.w ^ Thi);
        q0 = __uint_as_float(a.x << 8) + __uint_as_float(d.x << 8);
        q1 = __uint_as_float(a.z << 8) + __uint_as_float(d.z << 8);
        if (__all(bad == 0)) break;
      }
      q0 = sw32add(q0, q0); q1 = sw32add(q1, q1);
      if (l < 32) *(LAS unsigned*)(L + L_Q + (wv >> 1) * QSTR + (wv & 1) * 128 + l * 4) = pk(q0, q1);
      lds_arrive(L + L_FLG + 4, l);
    }
    STAMP(3);
    lds_wait_ge(L + L_FLG + 16, 1);
    u32x4 kf[4];
    {
      const int key = ((c & 3) << 2) | (c >> 2);
#pragma unroll
      for (int d = 0; d < 4; ++d) kf[d] = *(LAS u32x4*)(L + L_K + (16 * wv + c) * 256 + (((4 * d + g4) ^ key) << 4));
    }
    lds_wait_ge(L + L_FLG + 4, 8);
    STAMP(4);
    {
      f32x4 s = {0.f, 0.f, 0.f, 0.f};
#pragma unroll
      for (int d = 0; d < 4; ++d) s = mfma32(kf[d], *(LAS u32x4*)(L + L_Q + (c & 3) * QSTR + d * 64 + g4 * 16), s);
      if (c < 4) *(LAS f32x4*)(L + L_S + c * SSTR + (16 * wv + 4 * g4) * 4) = s;
      lds_arrive(L + L_FLG + 8, l);
    }
    lds_wait_ge(L + L_FLG + 8, 8);
    u32x4 pb[4];
    {
      constexpr float C2 = 0.08838834764831845f * 1.4426950408889634f;
      LAS char* sp = L + L_S + (c & 3) * SSTR + (c >> 2) * 128 + g4 * 32;
      const f32x4 s0 = *(LAS f32x4*)sp, s1 = *(LAS f32x4*)(sp + 16);
      float m = fmaxf(fmaxf(fmaxf(s0[0], s0[1]), fmaxf(s0[2], s0[3])), fmaxf(fmaxf(s1[0], s1[1]), fmaxf(s1[2], s1[3])));
      m = fmaxf(m, dpp<ROR4>(m)); m = fmaxf(m, dpp<ROR8>(m));
      m = rowsmax(m) * C2;
      float p[8];
#pragma unroll
      for (int k = 0; k < 4; ++k) { p[k] = __builtin_amdgcn_exp2f(fmaf(s0[k], C2, -m)); p[4 + k] = __builtin_amdgcn_exp2f(fmaf(s1[k], C2, -m)); }
      float sum = ((p[0] + p[1]) + (p[2] + p[3])) + ((p[4] + p[5]) + (p[6] + p[7]));
      sum += dpp<ROR4>(sum); sum += dpp<ROR8>(sum);
      sum = rowssum(sum);
      if (wv == 0 && l < 4) __builtin_amdgcn_raw_buffer_store_b64(u32x2{clean(m), clean(sum)}, rs, W_ML + ((g * 4 + l) * 32 + j) * 8, 0, AUX_SC1);
      const u32x4 pp = {pk(p[0], p[1]), pk(p[2], p[3]), pk(p[4], p[5]), pk(p[6], p[7])};
#pragma unroll
      for (int ks = 0; ks < 4; ++ks) { const bool on = (c >> 2) == ks; pb[ks] = u32x4{on ? pp.x : 0u, on ? pp.y : 0u, on ? pp.z : 0u, on ? pp.w : 0u}; }
    }
    const int q = c >> 2, p = c & 3;
    const unsigned ta0 = (8 * g4 + q) * 256 + (((2 * wv + (p >> 1)) ^ ((q << 2) | ((2 * g4) & 3))) << 4) + 8 * (p & 1);
    const unsigned ta1 = (8 * g4 + 4 + q) * 256 + (((2 * wv + (p >> 1)) ^ ((q << 2) | ((2 * g4 + 1) & 3))) << 4) + 8 * (p & 1);
    STAMP(5);
    bar();
    STAMP(6);
    {
      f32x4 o = {0.f, 0.f, 0.f, 0.f};
#pragma unroll
      for (int ks = 0; ks < 4; ++ks) o = mfma32(trd2(L + L_V + ks * 8192 + ta0, L + L_V + ks * 8192 + ta1), pb[ks], o);
#pragma unroll
      for (int r = 0; r < 4; ++r) { o[r] += dpp<ROR4>(o[r]); o[r] += dpp<ROR8>(o[r]); }
      if (c < 4) __builtin_amdgcn_raw_buffer_store_b128(u32x4{clean(o[0]), clean(o[1]), clean(o[2]), clean(o[3])}, rs,
                                                        W_O + ((g * 8 + 2 * c + (wv >> 2)) * 32 + j) * 256 + (wv & 3) * 64 + g4 * 16, 0, AUX_SC1);
    }
    STAMP(7);
    if (ODELAY) __builtin_amdgcn_s_sleep(ODELAY);
    {
      u32x4 v; u32x2 ml;
      _Pragma("unroll 1") for (int sp = 0; sp < SPIN_MAX; ++sp) {
        asm volatile("" ::: "memory");
        v = __builtin_amdgcn_raw_buffer_load_b128(rs, W_O + rbg * 8192 + wv * 1024 + l * 16, 0, AUX_SC1);
        ml = __builtin_amdgcn_raw_buffer_load_b64(rs, W_ML + ((g * 4 + (rbl >> 1)) * 32 + ((4 * wv + g4) ^ ((l & 7) << 2))) * 8, 0, AUX_SC1);
        const unsigned mx = max(max(max(v.x, v.y), max(v.z, v.w)), max(ml.x, ml.y));
        if (__all(mx != SENT)) break;
      }
      const float m = __uint_as_float(ml.x);
      float M = fmaxf(m, dpp<XOR1>(m)); M = fmaxf(M, dpp<XOR2>(M)); M = fmaxf(M, dpp<HMIR>(M));
      M = rowsmax(M);
      const float w = __builtin_amdgcn_exp2f(m - M);
      float Ls = w * __uint_as_float(ml.y);
      Ls += dpp<XOR1>(Ls); Ls += dpp<XOR2>(Ls); Ls += dpp<HMIR>(Ls);
      Ls = rowssum(Ls);
      const float f = dpp<BC0>(w) * __builtin_amdgcn_rcpf(Ls);
      const float z = tr4(f * __uint_as_float(v.x), f * __uint_as_float(v.y), f * __uint_as_float(v.z), f * __uint_as_float(v.w));
      *(LAS float*)(L + L_OP + wv * 256 + (4 * c + g4) * 4) = z;
    }
    STAMP(8);
    bar();
    STAMP(9);
    const float gate = *(LAS float*)(L + L_FLG);
    {
      float o = 0.f;
      {
        float t[8];
#pragma unroll
        for (int u = 0; u < 8; ++u) t[u] = *(LAS float*)(L + L_OP + u * 256 + l * 4);
        o = ((t[0] + t[1]) + (t[2] + t[3])) + ((t[4] + t[5]) + (t[6] + t[7]));
      }
      const unsigned tb1 = g4 == 3 ? ta0 : ta1;
      const u32x4 w2f = trd2(L + L_W2 + ta0, L + L_W2 + tb1);
      const unsigned cw = 2 * wv + (p >> 1);
      const unsigned wa = (16 * g4 + q) * 256 + 8 * (p & 1);
      const u32x4 wf0 = trd2(L + L_WO + wa + ((cw ^ (q << 2)) << 4), L + L_WO + wa + 1024 + ((cw ^ ((q << 2) | 1)) << 4));
      const u32x4 wf1 = trd2(L + L_WO + wa + 2048 + ((cw ^ ((q << 2) | 2)) << 4), L + L_WO + wa + 3072 + ((cw ^ ((q << 2) | 3)) << 4));
      const u32x4 hf = *(LAS u32x4*)(L + L_H + g4 * 16);
      const unsigned pr = pk(o, dpp<0x101>(o));
      const u32x4 o0 = {pr, dppu<0x102>(pr), dppu<0x104>(pr), dppu<0x106>(pr)};
      const u32x4 o1 = {dppu<0x108>(pr), dppu<0x10A>(pr), dppu<0x10C>(pr), dppu<0x10E>(pr)};
      const f32x4 z4 = {0.f, 0.f, 0.f, 0.f};
      const f32x4 fb = mfma32(w2f, hf, z4);
      const f32x4 fa = mfma32(wf1, o1, mfma32(wf0, o0, z4));
      unsigned r[4];
#pragma unroll
      for (int k = 0; k < 4; ++k) r[k] = __builtin_amdgcn_alignbit(Tlo, __float_as_uint(gate * fa[k] + (1.f - gate) * fb[k]), 8);
      if (c == 0) {
        const unsigned o3 = W_P3 + ((cb * 64 + rbg) * 128 + 16 * wv + 4 * g4) * 8;
        __builtin_amdgcn_raw_buffer_store_b128(u32x4{r[0], Thi, r[1], Thi}, rs, o3, 0, AUX_SC1);
        __builtin_amdgcn_raw_buffer_store_b128(u32x4{r[2], Thi, r[3], Thi}, rs, o3 + 16, 0, AUX_SC1);
      }
    }
    STAMP(10);
    if (b < 128 && wv == 1) {
      const u32x2 bb = *(const CAS u32x2*)(b2 + 4 * b);
      if (FDELAY) __builtin_amdgcn_s_sleep(FDELAY);
      u32x4 a, d;
      _Pragma("unroll 1") for (int sp = 0; sp < SPIN_MAX; ++sp) {
        asm volatile("" ::: "memory");
        const unsigned o = W_P3 + (((b >> 5) * 64 + l) * 128 + 4 * (b & 31)) * 8;
        a = __builtin_amdgcn_raw_buffer_load_b128(rs, o, 0, AUX_SC1); d = __builtin_amdgcn_raw_buffer_load_b128(rs, o + 16, 0, AUX_SC1);
        const unsigned T24 = Tlo << 24;
        const unsigned bad = (((a.x ^ T24) | (a.z ^ T24) | (d.x ^ T24) | (d.z ^ T24)) >> 24) | (a.y ^ Thi) | (a.w ^ Thi) | (d.y ^ Thi) | (d.w ^ Thi);
        if (__all(bad == 0)) break;
      }
      float z = tr4(__uint_as_float(a.x << 8), __uint_as_float(a.z << 8), __uint_as_float(d.x << 8), __uint_as_float(d.z << 8));
      z += dpp<ROR8>(z); z += dpp<ROR4>(z); z += dpp<ROR2>(z); z += dpp<ROR1>(z);
      const unsigned bw = g4 < 2 ? bb.x : bb.y;
      z += (1.f - gate) * ((g4 & 1) ? bfhi(bw) : bflo(bw));
      if (c == 0) *(GAS u16*)(out + 4 * b + g4) = (u16)pk(z, z);
      if (b == 0 && l == 0) __hip_atomic_store((GAS u64*)(ws + W_E), E, __ATOMIC_RELAXED, __HIP_MEMORY_SCOPE_AGENT);
      STAMP(11);
    }
  }
}

extern "C" void kernel_launch(void* const* d_in, const int* in_sizes, int n_in, void* d_out, int out_size, void* d_ws, size_t ws_size, hipStream_t stream) {
  fused_kernel<<<256, 640, 0, stream>>>((const u16*)d_in[0], (const u16*)d_in[1], (const u16*)d_in[2], (const u16*)d_in[3], (const u16*)d_in[4], (const u16*)d_in[6],
                                        (const u16*)d_in[7], (const u16*)d_in[8], (const u16*)d_in[9], (const u16*)d_in[10], (u16*)d_out, (char*)d_ws);
}
```

```cpp
#include <hip/hip_runtime.h>
#include <stdint.h>
typedef unsigned short u16;
#define STAMPS 0
constexpr unsigned W_STAMP = 2097152;

__global__ void __launch_bounds__(640) gated_mix_kernel(const u16* x, const u16* wq, const u16* kc, const u16* vc, char* ws, const u16* w1, const u16* gw,
                                                        const u16* b1, const u16* w2, const u16* wo, const u16* b2, u16* out) {
  __shared__ __attribute__((aligned(1024))) char smem[120640];
  asm volatile(
R"ASM(.Lgm_base:
s_load_dwordx8 s[8:15], s[0:1], 0x0
s_load_dwordx8 s[16:23], s[0:1], 0x20
s_load_dwordx8 s[24:31], s[0:1], 0x40
v_readfirstlane_b32 s3, v0
v_and_b32 v1, 63, v0
s_and_b32 s4, s2, 7
s_lshr_b32 s5, s2, 3
s_lshr_b32 s3, s3, 6
s_cmp_eq_u32 s3, 0
s_cbranch_scc1 .Lgm_w0
s_cmp_eq_u32 s3, 2
s_cbranch_scc1 .Lgm_ffn
s_cmp_lg_u32 s3, 1
s_cbranch_scc1 .Lgm_cpre
.Lgm_load:
s_setprio 2
v_lshrrev_b32 v2, 4, v1
v_and_b32 v3, 15, v1
v_lshlrev_b32 v4, 2, v2
v_xor_b32 v4, v4, v3
v_lshlrev_b32 v4, 4, v4
v_lshl_or_b32 v6, v2, 8, v4
v_xor_b32 v5, 16, v6
v_xor_b32 v8, 32, v6
v_xor_b32 v9, 48, v6
v_add_u32 v5, 0x7c00, v5
v_add_u32 v8, 0xf800, v8
v_add_u32 v9, 0x17400, v9
s_lshl_b32 s34, s4, 24
s_lshl_b32 s35, s5, 10
s_add_u32 s34, s34, s35
s_add_u32 s34, s34, 0xf00000
s_lshl_b32 s39, s4, 17
s_mov_b32 s38, 16384
v_lshl_or_b32 v10, v2, 10, v4
v_xor_b32 v11, 16, v10
v_xor_b32 v12, 32, v10
v_xor_b32 v13, 48, v10
v_add_u32 v11, 0xc00, v11
v_add_u32 v12, 0x1800, v12
v_add_u32 v13, 0x2400, v13
s_lshr_b32 s6, s5, 2
s_and_b32 s7, s5, 3
s_lshl_b32 s42, s4, 3
s_add_u32 s42, s42, s6
s_lshl_b32 s7, s7, 8
s_mul_i32 s35, s42, 0x7000
s_add_u32 s35, s35, s7
s_lshl_b32 s36, s4, 19
s_lshl_b32 s37, s6, 14
s_add_u32 s36, s36, s37
s_add_u32 s36, s36, s7
v_mov_b32 v30, 118400
s_waitcnt lgkmcnt(0)
s_load_dwordx2 s[32:33], s[16:17], 0x0
s_add_u32 s12, s12, s34
s_addc_u32 s13, s13, 0
s_add_u32 s14, s14, s34
s_addc_u32 s15, s15, 0
s_add_u32 s24, s24, s35
s_addc_u32 s25, s25, 0
s_add_u32 s26, s26, s36
s_addc_u32 s27, s27, 0
s_sleep 18
.Lgm_l0:
s_mov_b32 m0, s38
s_add_u32 s40, s12, s39
s_addc_u32 s41, s13, 0
s_add_u32 s39, s39, 0x20000
s_and_b32 s39, s39, 0xfffff
s_nop 11
global_load_lds_dwordx4 v6, s[40:41] nt
s_nop 11
global_load_lds_dwordx4 v5, s[40:41] offset:1024 nt
s_nop 11
global_load_lds_dwordx4 v8, s[40:41] offset:2048 nt
s_nop 11
global_load_lds_dwordx4 v9, s[40:41] offset:3072 nt
s_add_u32 s38, s38, 4096
s_cmp_lg_u32 s38, 36864
s_cbranch_scc1 .Lgm_l0
.Lgm_l1:
s_cmp_eq_u32 s38, 49152
s_cselect_b32 s12, s14, s12
s_cselect_b32 s13, s15, s13
s_mov_b32 m0, s38
s_add_u32 s40, s12, s39
s_addc_u32 s41, s13, 0
s_add_u32 s39, s39, 0x20000
s_and_b32 s39, s39, 0xfffff
s_waitcnt vmcnt(18)
global_load_lds_dwordx4 v6, s[40:41] nt
global_load_lds_dwordx4 v5, s[40:41] offset:1024 nt
s_waitcnt vmcnt(18)
global_load_lds_dwordx4 v8, s[40:41] offset:2048 nt
global_load_lds_dwordx4 v9, s[40:41] offset:3072 nt
s_cmp_eq_u32 s38, 65536
s_cbranch_scc1 .Lgm_kflag
.Lgm_l1b:
s_add_u32 s38, s38, 4096
s_cmp_lg_u32 s38, 81920
s_cbranch_scc1 .Lgm_l1
s_mov_b32 m0, s38
s_waitcnt vmcnt(18)
global_load_lds_dwordx4 v10, s[24:25] nt
global_load_lds_dwordx4 v11, s[24:25] offset:1024 nt
s_waitcnt vmcnt(18)
global_load_lds_dwordx4 v12, s[24:25] offset:2048 nt
global_load_lds_dwordx4 v13, s[24:25] offset:3072 nt
s_mov_b32 m0, 86016
s_add_u32 s24, s24, 16384
s_addc_u32 s25, s25, 0
s_mov_b32 s38, 89088
s_waitcnt vmcnt(18)
global_load_lds_dwordx4 v10, s[24:25] nt
global_load_lds_dwordx4 v11, s[24:25] offset:1024 nt
s_waitcnt vmcnt(18)
global_load_lds_dwordx4 v12, s[24:25] offset:2048 nt
.Lgm_l2:
s_mov_b32 m0, s38
s_add_u32 s38, s38, 4096
s_waitcnt vmcnt(18)
global_load_lds_dwordx4 v10, s[26:27] nt
global_load_lds_dwordx4 v11, s[26:27] offset:1024 nt
s_waitcnt vmcnt(18)
global_load_lds_dwordx4 v12, s[26:27] offset:2048 nt
global_load_lds_dwordx4 v13, s[26:27] offset:3072 nt
s_add_u32 s26, s26, 0x20000
s_addc_u32 s27, s27, 0
s_cmp_lg_u32 s38, 97280
s_cbranch_scc1 .Lgm_l2
s_waitcnt vmcnt(15)
s_barrier
.Lgm_l3:
s_mov_b32 m0, s38
s_add_u32 s38, s38, 4096
global_load_lds_dwordx4 v10, s[26:27] nt
global_load_lds_dwordx4 v11, s[26:27] offset:1024 nt
global_load_lds_dwordx4 v12, s[26:27] offset:2048 nt
global_load_lds_dwordx4 v13, s[26:27] offset:3072 nt
s_add_u32 s26, s26, 0x20000
s_addc_u32 s27, s27, 0
s_cmp_lg_u32 s38, 105472
s_cbranch_scc1 .Lgm_l3
s_waitcnt vmcnt(0)
s_barrier
s_endpgm
.Lgm_kflag:
s_waitcnt vmcnt(20) lgkmcnt(0)
s_add_u32 s32, s32, 1
s_addc_u32 s33, s33, 0
v_mov_b32 v64, s32
v_mov_b32 v65, s33
ds_write_b64 v30, v[64:65] offset:8
s_branch .Lgm_l1b
.Lgm_w0:
s_getpc_b64 s[36:37]
.Lgm_pc:
s_and_b32 s34, s5, 7
s_min_u32 s34, s34, (.Lgm_end-.Lgm_base)>>10
s_sub_u32 s36, s36, .Lgm_pc-.Lgm_base
s_subb_u32 s37, s37, 0
s_lshl_b32 s34, s34, 10
s_mov_b32 m0, 118592
v_lshl_add_u32 v12, v1, 4, s34
global_load_lds_dwordx4 v12, s[36:37]
s_setprio 3
v_lshrrev_b32 v6, 3, v1
v_and_b32 v7, 2, v6
v_and_b32 v8, 7, v1
v_xor_b32 v8, v8, v7
v_lshlrev_b32 v8, 4, v8
v_lshl_or_b32 v8, v6, 13, v8
v_xor_b32 v9, 64, v8
v_add_u32 v9, 0xfc00, v9
v_add_u32 v10, 0x1f800, v8
v_add_u32 v11, 0x1f800, v9
v_lshlrev_b32 v12, 2, v1
s_and_b32 s7, s5, 3
s_lshr_b32 s6, s5, 2
s_lshl_b32 s34, s7, 20
s_lshl_b32 s35, s4, 10
s_or_b32 s34, s34, s35
s_lshl_b32 s35, s6, 7
s_or_b32 s34, s34, s35
s_lshl_b32 s35, s7, 8
s_mov_b32 m0, 0
s_waitcnt lgkmcnt(0)
s_add_u32 s10, s10, s34
s_addc_u32 s11, s11, 0
global_load_lds_dwordx4 v8, s[10:11] nt
global_load_lds_dwordx4 v9, s[10:11] offset:1024 nt
global_load_lds_dwordx4 v10, s[10:11] offset:2048 nt
global_load_lds_dwordx4 v11, s[10:11] offset:3072 nt
s_load_dwordx2 s[32:33], s[16:17], 0x0
s_add_u32 s36, s8, s35
s_addc_u32 s37, s9, 0
s_mov_b32 m0, 112640
s_add_u32 s10, s10, 262144
s_addc_u32 s11, s11, 0
global_load_lds_dword v12, s[36:37]
s_mov_b32 m0, 4096
s_nop 0
global_load_lds_dwordx4 v8, s[10:11] nt
global_load_lds_dwordx4 v9, s[10:11] offset:1024 nt
global_load_lds_dwordx4 v10, s[10:11] offset:2048 nt
global_load_lds_dwordx4 v11, s[10:11] offset:3072 nt
s_mov_b32 m0, 8192
s_add_u32 s10, s10, 262144
s_addc_u32 s11, s11, 0
global_load_lds_dwordx4 v8, s[10:11] nt
global_load_lds_dwordx4 v9, s[10:11] offset:1024 nt
global_load_lds_dwordx4 v10, s[10:11] offset:2048 nt
global_load_lds_dwordx4 v11, s[10:11] offset:3072 nt
s_mov_b32 m0, 12288
s_add_u32 s10, s10, 262144
s_addc_u32 s11, s11, 0
global_load_lds_dwordx4 v8, s[10:11] nt
global_load_lds_dwordx4 v9, s[10:11] offset:1024 nt
global_load_lds_dwordx4 v10, s[10:11] offset:2048 nt
global_load_lds_dwordx4 v11, s[10:11] offset:3072 nt
s_and_b32 s34, s5, 7
s_lshl_b32 s34, s34, 10
s_lshl_b32 s35, s4, 13
s_add_u32 s34, s34, s35
s_add_u32 s34, s34, 4096
s_mov_b32 m0, 118592
v_lshl_add_u32 v12, v1, 4, s34
global_load_lds_dwordx4 v12, s[16:17] sc1
v_lshrrev_b32 v2, 4, v1
v_and_b32 v3, 15, v1
v_lshrrev_b32 v4, 2, v3
v_and_b32 v5, 3, v1
s_lshl_b32 s34, s4, 2
s_add_u32 s34, s34, s7
s_lshl_b32 s34, s34, 11
s_lshl_b32 s35, s6, 8
s_add_u32 s34, s34, s35
s_add_u32 s34, s34, 4096
v_lshlrev_b32 v31, 6, v3
v_lshl_or_b32 v31, v2, 4, v31
v_add_u32 v31, s34, v31
v_lshrrev_b32 v32, 1, v4
v_and_b32 v33, 1, v2
v_lshl_or_b32 v32, v33, 1, v32
v_lshlrev_b32 v32, 1, v32
v_lshrrev_b32 v33, 1, v5
v_xor_b32 v32, v32, v33
v_lshlrev_b32 v32, 4, v32
v_and_b32 v33, 1, v1
v_lshl_or_b32 v32, v33, 3, v32
v_lshl_or_b32 v32, v4, 7, v32
v_lshl_or_b32 v32, v2, 10, v32
v_xor_b32 v33, 32, v32
v_xor_b32 v34, 64, v32
v_xor_b32 v35, 0x60, v32
v_lshlrev_b32 v36, 4, v2
v_add_u32 v36, 112640, v36
v_cmp_eq_u32 s[52:53], 1, v3
v_cmp_eq_u32 s[54:55], 2, v3
v_cmp_eq_u32 s[56:57], 3, v3
v_cmp_gt_u32 s[44:45], 4, v3
v_mov_b32 v7, 0x3ff
v_mov_b32 v30, 118400
s_waitcnt lgkmcnt(0)
s_add_u32 s32, s32, 1
s_addc_u32 s33, s33, 0
s_and_b32 s48, s32, 0x3ff
s_lshr_b32 s49, s32, 10
s_lshr_b32 s50, s32, 20
s_lshr_b64 s[40:41], s[32:33], 30
s_xor_b32 s49, s49, s32
s_xor_b32 s50, s50, s32
s_xor_b32 s51, s40, s32
s_and_b32 s49, s49, 0x3ff
s_and_b32 s50, s50, 0x3ff
s_and_b32 s51, s51, 0x3ff
v_mov_b32 v64, s32
v_mov_b32 v65, s33
s_waitcnt vmcnt(13)
ds_read_b128 v[84:87], v36 offset:0
ds_read_b128 v[88:91], v36 offset:64
ds_read_b128 v[92:95], v36 offset:128
ds_read_b128 v[96:99], v36 offset:192
ds_read_b64_tr_b16 v[68:69], v32 offset:0
ds_read_b64_tr_b16 v[70:71], v32 offset:512
ds_read_b64_tr_b16 v[72:73], v33 offset:0
ds_read_b64_tr_b16 v[74:75], v33 offset:512
ds_read_b64_tr_b16 v[76:77], v34 offset:0
ds_read_b64_tr_b16 v[78:79], v34 offset:512
ds_read_b64_tr_b16 v[80:81], v35 offset:0
ds_read_b64_tr_b16 v[82:83], v35 offset:512
s_waitcnt lgkmcnt(6)
)ASM"
R"ASM(v_mfma_f32_16x16x32_bf16 v[40:43], v[68:71], v[84:87], 0
s_waitcnt lgkmcnt(4)
v_mfma_f32_16x16x32_bf16 v[44:47], v[72:75], v[84:87], 0
s_waitcnt lgkmcnt(2)
v_mfma_f32_16x16x32_bf16 v[48:51], v[76:79], v[84:87], 0
s_waitcnt lgkmcnt(0)
v_mfma_f32_16x16x32_bf16 v[52:55], v[80:83], v[84:87], 0
s_waitcnt vmcnt(9)
ds_read_b64_tr_b16 v[68:69], v32 offset:4096
ds_read_b64_tr_b16 v[70:71], v32 offset:4608
ds_read_b64_tr_b16 v[72:73], v33 offset:4096
ds_read_b64_tr_b16 v[74:75], v33 offset:4608
ds_read_b64_tr_b16 v[76:77], v34 offset:4096
ds_read_b64_tr_b16 v[78:79], v34 offset:4608
ds_read_b64_tr_b16 v[80:81], v35 offset:4096
ds_read_b64_tr_b16 v[82:83], v35 offset:4608
s_waitcnt lgkmcnt(6)
v_mfma_f32_16x16x32_bf16 v[40:43], v[68:71], v[88:91], v[40:43]
s_waitcnt lgkmcnt(4)
v_mfma_f32_16x16x32_bf16 v[44:47], v[72:75], v[88:91], v[44:47]
s_waitcnt lgkmcnt(2)
v_mfma_f32_16x16x32_bf16 v[48:51], v[76:79], v[88:91], v[48:51]
s_waitcnt lgkmcnt(0)
v_mfma_f32_16x16x32_bf16 v[52:55], v[80:83], v[88:91], v[52:55]
s_waitcnt vmcnt(5)
ds_read_b64_tr_b16 v[68:69], v32 offset:8192
ds_read_b64_tr_b16 v[70:71], v32 offset:8704
ds_read_b64_tr_b16 v[72:73], v33 offset:8192
ds_read_b64_tr_b16 v[74:75], v33 offset:8704
ds_read_b64_tr_b16 v[76:77], v34 offset:8192
ds_read_b64_tr_b16 v[78:79], v34 offset:8704
ds_read_b64_tr_b16 v[80:81], v35 offset:8192
ds_read_b64_tr_b16 v[82:83], v35 offset:8704
s_waitcnt lgkmcnt(6)
v_mfma_f32_16x16x32_bf16 v[40:43], v[68:71], v[92:95], v[40:43]
s_waitcnt lgkmcnt(4)
v_mfma_f32_16x16x32_bf16 v[44:47], v[72:75], v[92:95], v[44:47]
s_waitcnt lgkmcnt(2)
v_mfma_f32_16x16x32_bf16 v[48:51], v[76:79], v[92:95], v[48:51]
s_waitcnt lgkmcnt(0)
v_mfma_f32_16x16x32_bf16 v[52:55], v[80:83], v[92:95], v[52:55]
s_waitcnt vmcnt(1)
ds_read_b64_tr_b16 v[68:69], v32 offset:12288
ds_read_b64_tr_b16 v[70:71], v32 offset:12800
ds_read_b64_tr_b16 v[72:73], v33 offset:12288
ds_read_b64_tr_b16 v[74:75], v33 offset:12800
ds_read_b64_tr_b16 v[76:77], v34 offset:12288
ds_read_b64_tr_b16 v[78:79], v34 offset:12800
ds_read_b64_tr_b16 v[80:81], v35 offset:12288
ds_read_b64_tr_b16 v[82:83], v35 offset:12800
s_waitcnt lgkmcnt(6)
v_mfma_f32_16x16x32_bf16 v[40:43], v[68:71], v[96:99], v[40:43]
s_waitcnt lgkmcnt(4)
v_mfma_f32_16x16x32_bf16 v[44:47], v[72:75], v[96:99], v[44:47]
s_waitcnt lgkmcnt(2)
v_mfma_f32_16x16x32_bf16 v[48:51], v[76:79], v[96:99], v[48:51]
s_waitcnt lgkmcnt(0)
v_mfma_f32_16x16x32_bf16 v[52:55], v[80:83], v[96:99], v[52:55]
s_mov_b64 exec, s[44:45]
s_nop 3
v_cndmask_b32 v40, v40, v44, s[52:53]
v_cndmask_b32 v41, v41, v45, s[52:53]
v_cndmask_b32 v42, v42, v46, s[52:53]
v_cndmask_b32 v43, v43, v47, s[52:53]
v_cndmask_b32 v40, v40, v48, s[54:55]
v_cndmask_b32 v41, v41, v49, s[54:55]
v_cndmask_b32 v42, v42, v50, s[54:55]
v_cndmask_b32 v43, v43, v51, s[54:55]
v_cndmask_b32 v40, v40, v52, s[56:57]
v_cndmask_b32 v41, v41, v53, s[56:57]
v_cndmask_b32 v42, v42, v54, s[56:57]
v_cndmask_b32 v43, v43, v55, s[56:57]
v_bfi_b32 v56, v7, s48, v40
v_bfi_b32 v57, v7, s49, v41
v_bfi_b32 v58, v7, s50, v42
v_bfi_b32 v59, v7, s51, v43
global_store_dwordx4 v31, v[56:59], s[16:17] sc1
s_mov_b64 exec, -1
ds_write_b64 v30, v[64:65] offset:16
s_setprio 0
s_branch .Lgm_cset
.Lgm_cpre:
s_sub_u32 s3, s3, 2
v_mov_b32 v30, 118400
v_lshrrev_b32 v2, 4, v1
v_and_b32 v3, 15, v1
v_lshrrev_b32 v4, 2, v3
v_and_b32 v5, 3, v1
s_lshr_b32 s6, s5, 2
s_and_b32 s7, s5, 3
v_cmp_gt_u32 s[44:45], 4, v3
v_mov_b32 v7, 0x3ff
s_waitcnt lgkmcnt(0)
s_load_dwordx2 s[32:33], s[16:17], 0x0
.Lgm_cset:
s_lshl_b32 s42, s4, 3
s_add_u32 s42, s42, s6
s_lshl_b32 s34, s4, 13
s_lshl_b32 s35, s3, 8
s_add_u32 s34, s34, s35
s_add_u32 s34, s34, 4096
v_lshlrev_b32 v6, 11, v2
v_lshl_or_b32 v6, v3, 4, v6
v_add_u32 v6, s34, v6
s_lshr_b32 s34, s3, 1
s_mul_i32 s34, s34, 320
s_and_b32 s35, s3, 1
s_lshl_b32 s35, s35, 7
s_add_u32 s34, s34, s35
s_add_u32 s34, s34, 112896
v_lshlrev_b32 v8, 3, v3
v_lshl_or_b32 v8, v2, 1, v8
v_add_u32 v8, s34, v8
v_lshl_or_b32 v9, v5, 2, v4
v_xor_b32 v9, v9, v2
v_lshlrev_b32 v9, 4, v9
v_lshl_or_b32 v9, v3, 8, v9
s_lshl_b32 s34, s3, 12
s_add_u32 s34, s34, 16384
v_add_u32 v9, s34, v9
v_xor_b32 v10, 64, v9
v_xor_b32 v11, 0x80, v9
v_xor_b32 v12, 0xc0, v9
v_mul_u32_u24 v13, 320, v5
v_lshl_add_u32 v13, v2, 4, v13
v_add_u32 v13, 112896, v13
v_mul_u32_u24 v14, 528, v3
v_lshl_add_u32 v14, v2, 4, v14
s_lshl_b32 s34, s3, 6
s_add_u32 s34, s34, 114176
v_add_u32 v14, s34, v14
v_mul_u32_u24 v15, 528, v5
v_lshl_add_u32 v15, v4, 7, v15
v_lshl_add_u32 v15, v2, 5, v15
v_add_u32 v15, 114176, v15
v_lshrrev_b32 v23, 1, v5
s_lshl_b32 s34, s3, 1
v_or_b32 v23, s34, v23
v_lshlrev_b32 v24, 2, v4
v_xor_b32 v23, v23, v24
v_and_b32 v24, 1, v1
v_lshlrev_b32 v24, 3, v24
v_lshl_or_b32 v24, v4, 8, v24
v_lshlrev_b32 v25, 1, v2
v_and_b32 v25, 2, v25
v_xor_b32 v25, v25, v23
v_lshl_or_b32 v16, v25, 4, v24
v_lshl_add_u32 v16, v2, 11, v16
v_add_u32 v16, 49152, v16
v_xor_b32 v17, 16, v16
v_add_u32 v17, 0x400, v17
v_cmp_eq_u32 vcc, 3, v2
v_lshl_or_b32 v19, v23, 4, v24
v_lshl_add_u32 v19, v2, 12, v19
v_cndmask_b32 v18, v17, v16, vcc
v_add_u32 v19, 89088, v19
v_xor_b32 v20, 16, v19
v_xor_b32 v21, 32, v19
v_xor_b32 v22, 48, v19
s_lshl_b32 s34, s4, 3
s_add_u32 s34, s34, s3
s_lshl_b32 s34, s34, 13
s_lshl_b32 s35, s5, 8
s_add_u32 s34, s34, s35
s_add_u32 s34, s34, 69632
v_lshlrev_b32 v23, 6, v3
v_lshl_or_b32 v23, v2, 4, v23
v_add_u32 v23, s34, v23
s_lshl_b32 s34, s42, 13
s_lshl_b32 s35, s3, 10
s_add_u32 s34, s34, s35
s_add_u32 s34, s34, 69632
v_lshlrev_b32 v24, 4, v1
v_add_u32 v24, s34, v24
s_lshl_b32 s34, s4, 12
s_add_u32 s34, s34, 593920
s_lshl_b32 s35, s3, 2
v_lshl_or_b32 v25, v5, 2, v2
v_xor_b32 v25, s35, v25
v_lshlrev_b32 v25, 7, v25
v_lshl_or_b32 v25, v4, 4, v25
v_xor_b32 v26, 0x800, v25
v_add_u32 v25, s34, v25
v_add_u32 v26, s34, v26
s_lshl_b32 s35, s5, 7
s_add_u32 s34, s34, s35
v_lshl_add_u32 v31, v1, 4, s34
s_lshl_b32 s34, s3, 8
s_add_u32 s34, s34, 116288
v_lshlrev_b32 v27, 4, v3
v_lshl_or_b32 v27, v2, 2, v27
v_add_u32 v27, s34, v27
v_lshlrev_b32 v28, 2, v1
v_add_u32 v28, 116288, v28
s_lshl_b32 s34, s7, 6
s_add_u32 s34, s34, s42
s_lshl_b32 s34, s34, 10
s_lshl_b32 s35, s3, 7
s_add_u32 s34, s34, s35
s_add_u32 s34, s34, 888832
v_lshlrev_b32 v29, 4, v2
v_lshl_or_b32 v29, v3, 6, v29
v_add_u32 v29, s34, v29
v_cmp_gt_u32 s[46:47], 2, v3
v_lshlrev_b32 v63, 4, v2
v_add_u32 v63, 118336, v63
s_lshl_b32 s34, s3, 3
v_add_u32 v62, s34, v30
v_and_b32 v103, 7, v1
v_lshl_add_u32 v103, v103, 3, v30
s_cmp_eq_u32 s3, 0
s_cbranch_scc1 .Lgm_sp0_d
s_waitcnt lgkmcnt(0)
s_add_u32 s32, s32, 1
s_addc_u32 s33, s33, 0
s_and_b32 s48, s32, 0x3ff
s_lshr_b32 s49, s32, 10
s_lshr_b32 s50, s32, 20
s_lshr_b64 s[40:41], s[32:33], 30
s_xor_b32 s49, s49, s32
s_xor_b32 s50, s50, s32
s_xor_b32 s51, s40, s32
s_and_b32 s49, s49, 0x3ff
s_and_b32 s50, s50, 0x3ff
s_and_b32 s51, s51, 0x3ff
v_mov_b32 v64, s32
v_mov_b32 v65, s33
s_mov_b32 s38, 0x400000
.Lgm_sp0:
ds_read_b64 v[66:67], v30 offset:16
s_waitcnt lgkmcnt(0)
v_cmp_ne_u64 vcc, v[66:67], v[64:65]
s_cbranch_vccz .Lgm_sp0_d
s_sleep 1
s_sub_u32 s38, s38, 1
s_cbranch_scc0 .Lgm_sp0
.Lgm_sp0_d:
s_sleep 18
s_mov_b32 s43, 131072
.Lgm_qpoll:
global_load_dwordx4 v[32:35], v6, s[16:17] sc1
s_waitcnt vmcnt(0)
v_xor_b32 v32, s48, v32
v_xor_b32 v33, s49, v33
v_xor_b32 v34, s50, v34
v_xor_b32 v35, s51, v35
v_or3_b32 v40, v32, v33, v34
v_or_b32 v40, v40, v35
v_and_b32 v40, v7, v40
v_cmp_ne_u32 vcc, 0, v40
s_cbranch_vccnz .Lgm_qretry
.Lgm_qok:
v_permlane32_swap_b32 v32, v34
v_permlane32_swap_b32 v33, v35
v_add_f32 v32, v32, v34
v_add_f32 v33, v33, v35
s_nop 1
v_permlane16_swap_b32 v32, v33
v_add_f32 v32, v32, v33
v_cvt_pk_bf16_f32 v32, v32, v32
ds_write_b16 v8, v32
ds_write_b64 v62, v[64:65] offset:64
s_mov_b32 s38, 0x400000
.Lgm_sp1:
ds_read_b64 v[66:67], v30 offset:8
s_waitcnt lgkmcnt(0)
v_cmp_ne_u64 vcc, v[66:67], v[64:65]
s_cbranch_vccz .Lgm_sp1_d
s_sleep 1
s_sub_u32 s38, s38, 1
s_cbranch_scc0 .Lgm_sp1
.Lgm_sp1_d:
ds_read_b128 v[68:71], v9
ds_read_b128 v[72:75], v10
ds_read_b128 v[76:79], v11
ds_read_b128 v[80:83], v12
s_mov_b32 s38, 0x400000
.Lgm_sp2:
)ASM"
R"ASM(ds_read_b64 v[66:67], v103 offset:64
s_waitcnt lgkmcnt(0)
v_cmp_ne_u64 vcc, v[66:67], v[64:65]
s_cbranch_vccz .Lgm_sp2_d
s_sleep 1
s_sub_u32 s38, s38, 1
s_cbranch_scc0 .Lgm_sp2
.Lgm_sp2_d:
ds_read_b128 v[84:87], v13
ds_read_b128 v[88:91], v13 offset:64
ds_read_b128 v[92:95], v13 offset:128
ds_read_b128 v[96:99], v13 offset:192
s_waitcnt lgkmcnt(3)
v_mfma_f32_16x16x32_bf16 v[32:35], v[68:71], v[84:87], 0
s_waitcnt lgkmcnt(2)
v_mfma_f32_16x16x32_bf16 v[32:35], v[72:75], v[88:91], v[32:35]
s_waitcnt lgkmcnt(1)
v_mfma_f32_16x16x32_bf16 v[32:35], v[76:79], v[92:95], v[32:35]
s_waitcnt lgkmcnt(0)
v_mfma_f32_16x16x32_bf16 v[32:35], v[80:83], v[96:99], v[32:35]
s_mov_b64 exec, s[44:45]
s_nop 6
ds_write_b128 v14, v[32:35]
ds_write_b64 v62, v[64:65] offset:128
s_mov_b64 exec, -1
s_mov_b32 s38, 0x400000
.Lgm_sp3:
ds_read_b64 v[66:67], v103 offset:128
s_waitcnt lgkmcnt(0)
v_cmp_ne_u64 vcc, v[66:67], v[64:65]
s_cbranch_vccz .Lgm_sp3_d
s_sleep 1
s_sub_u32 s38, s38, 1
s_cbranch_scc0 .Lgm_sp3
.Lgm_sp3_d:
ds_read_b128 v[32:35], v15
ds_read_b128 v[36:39], v15 offset:16
s_mov_b32 s34, 1040356334
s_waitcnt lgkmcnt(0)
v_max3_f32 v40, v32, v33, v34
v_max3_f32 v41, v35, v36, v37
v_max3_f32 v40, v38, v39, v40
v_max_f32 v40, v40, v41
s_nop 1
v_max_f32_dpp v40, v40, v40 row_ror:4 row_mask:0xf bank_mask:0xf
s_nop 1
v_max_f32_dpp v40, v40, v40 row_ror:8 row_mask:0xf bank_mask:0xf
v_mov_b32 v41, v40
s_nop 1
v_permlane16_swap_b32 v40, v41
v_max_f32 v40, v40, v41
v_mov_b32 v41, v40
s_nop 1
v_permlane32_swap_b32 v40, v41
v_max_f32 v40, v40, v41
v_mul_f32 v60, s34, v40
v_fma_f32 v32, v32, s34, -v60
v_fma_f32 v33, v33, s34, -v60
v_fma_f32 v34, v34, s34, -v60
v_fma_f32 v35, v35, s34, -v60
v_fma_f32 v36, v36, s34, -v60
v_fma_f32 v37, v37, s34, -v60
v_fma_f32 v38, v38, s34, -v60
v_fma_f32 v39, v39, s34, -v60
v_exp_f32 v32, v32
v_exp_f32 v33, v33
v_exp_f32 v34, v34
v_exp_f32 v35, v35
v_exp_f32 v36, v36
v_exp_f32 v37, v37
v_exp_f32 v38, v38
v_exp_f32 v39, v39
s_nop 0
v_cvt_pk_bf16_f32 v68, v32, v33
v_cvt_pk_bf16_f32 v69, v34, v35
v_cvt_pk_bf16_f32 v70, v36, v37
v_cvt_pk_bf16_f32 v71, v38, v39
s_cmp_lg_u32 s3, 0
v_mov_b32_dpp v72, v68 row_shl:4 row_mask:0xf bank_mask:0xf bound_ctrl:1
v_mov_b32_dpp v73, v69 row_shl:4 row_mask:0xf bank_mask:0xf bound_ctrl:1
v_mov_b32_dpp v74, v70 row_shl:4 row_mask:0xf bank_mask:0xf bound_ctrl:1
v_mov_b32_dpp v75, v71 row_shl:4 row_mask:0xf bank_mask:0xf bound_ctrl:1
v_mov_b32_dpp v76, v68 row_shl:8 row_mask:0xf bank_mask:0xf bound_ctrl:1
v_mov_b32_dpp v77, v69 row_shl:8 row_mask:0xf bank_mask:0xf bound_ctrl:1
v_mov_b32_dpp v78, v70 row_shl:8 row_mask:0xf bank_mask:0xf bound_ctrl:1
v_mov_b32_dpp v79, v71 row_shl:8 row_mask:0xf bank_mask:0xf bound_ctrl:1
v_mov_b32_dpp v80, v68 row_shl:12 row_mask:0xf bank_mask:0xf bound_ctrl:1
v_mov_b32_dpp v81, v69 row_shl:12 row_mask:0xf bank_mask:0xf bound_ctrl:1
v_mov_b32_dpp v82, v70 row_shl:12 row_mask:0xf bank_mask:0xf bound_ctrl:1
v_mov_b32_dpp v83, v71 row_shl:12 row_mask:0xf bank_mask:0xf bound_ctrl:1
s_cbranch_scc1 .Lgm_noml
v_add_f32 v32, v32, v33
v_add_f32 v34, v34, v35
v_add_f32 v36, v36, v37
v_add_f32 v38, v38, v39
v_add_f32 v32, v32, v34
v_add_f32 v36, v36, v38
v_add_f32 v32, v32, v36
s_nop 1
v_add_f32_dpp v32, v32, v32 row_ror:4 row_mask:0xf bank_mask:0xf
s_nop 1
v_add_f32_dpp v32, v32, v32 row_ror:8 row_mask:0xf bank_mask:0xf
v_mov_b32 v33, v32
s_nop 1
v_permlane16_swap_b32 v32, v33
v_add_f32 v32, v32, v33
v_mov_b32 v33, v32
s_nop 1
v_permlane32_swap_b32 v32, v33
v_add_f32 v32, v32, v33
v_bfi_b32 v52, v7, s48, v60
v_lshl_or_b32 v53, v60, 22, s49
v_bfi_b32 v54, v7, s50, v32
v_mov_b32 v55, s51
s_mov_b64 exec, 0xff
global_store_dwordx4 v31, v[52:55], s[16:17] sc1
s_mov_b64 exec, -1
.Lgm_noml:
s_barrier
ds_read_b64_tr_b16 v[84:85], v16 offset:0
ds_read_b64_tr_b16 v[86:87], v17 offset:0
ds_read_b64_tr_b16 v[88:89], v16 offset:8192
ds_read_b64_tr_b16 v[90:91], v17 offset:8192
ds_read_b64_tr_b16 v[92:93], v16 offset:16384
ds_read_b64_tr_b16 v[94:95], v17 offset:16384
ds_read_b64_tr_b16 v[96:97], v16 offset:24576
ds_read_b64_tr_b16 v[98:99], v17 offset:24576
ds_read_b32 v61, v30
s_waitcnt lgkmcnt(7)
v_mfma_f32_16x16x32_bf16 v[32:35], v[84:87], v[68:71], 0
s_waitcnt lgkmcnt(5)
v_mfma_f32_16x16x32_bf16 v[32:35], v[88:91], v[72:75], v[32:35]
s_waitcnt lgkmcnt(3)
v_mfma_f32_16x16x32_bf16 v[32:35], v[92:95], v[76:79], v[32:35]
s_waitcnt lgkmcnt(1)
v_mfma_f32_16x16x32_bf16 v[32:35], v[96:99], v[80:83], v[32:35]
s_mov_b64 exec, s[44:45]
s_mov_b32 s43, 131072
s_nop 5
v_bfi_b32 v32, v7, s48, v32
v_bfi_b32 v33, v7, s49, v33
v_bfi_b32 v34, v7, s50, v34
v_bfi_b32 v35, v7, s51, v35
global_store_dwordx4 v23, v[32:35], s[16:17] sc1
s_mov_b64 exec, -1
s_sleep 8
global_load_dwordx4 v[48:51], v25, s[16:17] sc1
global_load_dwordx4 v[52:55], v26, s[16:17] sc1
global_load_dwordx4 v[36:39], v24, s[16:17] sc1
s_waitcnt vmcnt(1)
.Lgm_mlchk:
v_xor_b32 v48, s48, v48
v_xor_b32 v49, s49, v49
v_xor_b32 v50, s50, v50
v_xor_b32 v51, s51, v51
v_xor_b32 v52, s48, v52
v_xor_b32 v53, s49, v53
v_xor_b32 v54, s50, v54
v_xor_b32 v55, s51, v55
v_or3_b32 v40, v48, v49, v50
v_or3_b32 v40, v40, v51, v52
v_or3_b32 v40, v40, v53, v54
v_or_b32 v40, v40, v55
v_and_b32 v40, v7, v40
v_cmp_ne_u32 vcc, 0, v40
s_cbranch_vccnz .Lgm_mlretry
.Lgm_mlok:
v_lshrrev_b32 v49, 22, v49
v_lshrrev_b32 v53, 22, v53
v_or_b32 v48, v49, v48
v_or_b32 v52, v53, v52
v_max_f32 v40, v48, v52
s_nop 1
v_max_f32_dpp v40, v40, v40 quad_perm:[1,0,3,2] row_mask:0xf bank_mask:0xf
s_nop 1
v_max_f32_dpp v40, v40, v40 quad_perm:[2,3,0,1] row_mask:0xf bank_mask:0xf
v_mov_b32 v41, v40
s_nop 1
v_permlane16_swap_b32 v40, v41
v_max_f32 v40, v40, v41
v_mov_b32 v41, v40
s_nop 1
v_permlane32_swap_b32 v40, v41
v_max_f32 v40, v40, v41
v_sub_f32 v42, v48, v40
v_sub_f32 v40, v52, v40
v_exp_f32 v42, v42
v_exp_f32 v40, v40
v_mul_f32 v41, v42, v50
v_fmac_f32 v41, v40, v54
s_waitcnt lgkmcnt(0)
v_mul_f32_dpp v43, v42, v61 quad_perm:[0,0,0,0] row_mask:0xf bank_mask:0xf
v_add_f32_dpp v41, v41, v41 quad_perm:[1,0,3,2] row_mask:0xf bank_mask:0xf
s_nop 1
v_add_f32_dpp v41, v41, v41 quad_perm:[2,3,0,1] row_mask:0xf bank_mask:0xf
v_mov_b32 v42, v41
s_nop 1
v_permlane16_swap_b32 v41, v42
v_add_f32 v41, v41, v42
v_mov_b32 v42, v41
s_nop 1
v_permlane32_swap_b32 v41, v42
v_add_f32 v41, v41, v42
v_rcp_f32 v41, v41
s_nop 0
v_mul_f32 v43, v43, v41
s_waitcnt vmcnt(0)
.Lgm_ochk:
v_xor_b32 v36, s48, v36
v_xor_b32 v37, s49, v37
v_xor_b32 v38, s50, v38
v_xor_b32 v39, s51, v39
v_or3_b32 v44, v36, v37, v38
v_or_b32 v44, v44, v39
v_and_b32 v44, v7, v44
v_cmp_ne_u32 vcc, 0, v44
s_cbranch_vccnz .Lgm_oretry
.Lgm_ook:
v_mul_f32 v36, v43, v36
v_mul_f32 v38, v43, v38
v_mul_f32 v37, v43, v37
v_mul_f32 v39, v43, v39
s_nop 0
v_permlane32_swap_b32 v36, v38
v_permlane32_swap_b32 v37, v39
v_add_f32 v36, v36, v38
v_add_f32 v37, v37, v39
s_nop 1
v_permlane16_swap_b32 v36, v37
v_add_f32 v36, v36, v37
ds_write_b32 v27, v36
s_waitcnt lgkmcnt(0)
s_barrier
ds_read2st64_b32 v[32:33], v28 offset0:0 offset1:1
ds_read2st64_b32 v[34:35], v28 offset0:2 offset1:3
ds_read2st64_b32 v[36:37], v28 offset0:4 offset1:5
ds_read2st64_b32 v[38:39], v28 offset0:6 offset1:7
ds_read_b128 v[44:47], v63
ds_read_b64_tr_b16 v[68:69], v16 offset:32768
ds_read_b64_tr_b16 v[70:71], v18 offset:32768
ds_read_b64_tr_b16 v[72:73], v19
ds_read_b64_tr_b16 v[74:75], v20 offset:1024
ds_read_b64_tr_b16 v[76:77], v21 offset:2048
ds_read_b64_tr_b16 v[78:79], v22 offset:3072
s_waitcnt lgkmcnt(9)
v_add_f32 v32, v32, v33
v_add_f32 v34, v34, v35
s_waitcnt lgkmcnt(7)
v_add_f32 v36, v36, v37
v_add_f32 v38, v38, v39
v_add_f32 v32, v32, v34
v_add_f32 v36, v36, v38
v_add_f32 v32, v32, v36
s_waitcnt lgkmcnt(4)
v_mfma_f32_16x16x32_bf16 v[40:43], v[68:71], v[44:47], 0
v_mov_b32_dpp v33, v32 row_shl:1 row_mask:0xf bank_mask:0xf bound_ctrl:1
v_cvt_pk_bf16_f32 v33, v32, v33
s_nop 1
v_mov_b32_dpp v80, v33 row_newbcast:0 row_mask:0xf bank_mask:0xf
v_mov_b32_dpp v81, v33 row_newbcast:2 row_mask:0xf bank_mask:0xf
v_mov_b32_dpp v82, v33 row_newbcast:4 row_mask:0xf bank_mask:0xf
v_mov_b32_dpp v83, v33 row_newbcast:6 row_mask:0xf bank_mask:0xf
)ASM"
R"ASM(v_mov_b32_dpp v84, v33 row_newbcast:8 row_mask:0xf bank_mask:0xf
v_mov_b32_dpp v85, v33 row_newbcast:10 row_mask:0xf bank_mask:0xf
v_mov_b32_dpp v86, v33 row_newbcast:12 row_mask:0xf bank_mask:0xf
v_mov_b32_dpp v87, v33 row_newbcast:14 row_mask:0xf bank_mask:0xf
s_waitcnt lgkmcnt(0)
v_mfma_f32_16x16x32_bf16 v[40:43], v[72:75], v[80:83], v[40:43]
v_mfma_f32_16x16x32_bf16 v[40:43], v[76:79], v[84:87], v[40:43]
s_mov_b64 exec, s[46:47]
s_nop 6
v_bfi_b32 v40, v7, s48, v40
v_bfi_b32 v41, v7, s49, v41
v_bfi_b32 v42, v7, s50, v42
v_bfi_b32 v43, v7, s51, v43
global_store_dwordx4 v29, v[40:43], s[16:17] sc1
s_endpgm
.Lgm_qretry:
s_sleep 2
s_sub_u32 s43, s43, 1
s_cbranch_scc0 .Lgm_qpoll
s_branch .Lgm_qok
.Lgm_mlretry:
s_sleep 2
s_sub_u32 s43, s43, 1
s_cbranch_scc1 .Lgm_mlok
global_load_dwordx4 v[48:51], v25, s[16:17] sc1
global_load_dwordx4 v[52:55], v26, s[16:17] sc1
s_waitcnt vmcnt(0)
s_branch .Lgm_mlchk
.Lgm_oretry:
s_sleep 2
s_sub_u32 s43, s43, 1
s_cbranch_scc1 .Lgm_ook
global_load_dwordx4 v[36:39], v24, s[16:17] sc1
s_waitcnt vmcnt(0)
s_branch .Lgm_ochk
.Lgm_ffn:
s_mov_b32 s3, 9
v_lshrrev_b32 v2, 4, v1
v_and_b32 v3, 15, v1
v_mul_u32_u24 v8, 37, v1
v_lshrrev_b32 v8, 10, v8
v_mul_u32_u24 v9, 28, v8
v_sub_u32 v9, v1, v9
s_mul_i32 s34, s5, 0xe000
s_mul_i32 s35, s4, 0x1c0
s_add_u32 s34, s34, s35
v_mul_u32_u24 v20, 0xe00, v8
v_lshl_add_u32 v20, v9, 4, v20
v_add_u32 v9, 8, v9
v_cmp_lt_u32 vcc, 27, v9
v_subrev_u32 v11, 28, v9
v_add_u32 v8, 2, v8
v_cndmask_b32 v9, v9, v11, vcc
v_addc_co_u32 v8, vcc, 0, v8, vcc
v_mul_u32_u24 v21, 0xe00, v8
v_lshl_add_u32 v21, v9, 4, v21
v_subrev_u32 v21, 1024, v21
v_add_u32 v9, 8, v9
v_cmp_lt_u32 vcc, 27, v9
v_subrev_u32 v11, 28, v9
v_add_u32 v8, 2, v8
v_cndmask_b32 v9, v9, v11, vcc
v_addc_co_u32 v8, vcc, 0, v8, vcc
v_mul_u32_u24 v22, 0xe00, v8
v_lshl_add_u32 v22, v9, 4, v22
v_subrev_u32 v22, 2048, v22
v_add_u32 v9, 8, v9
v_cmp_lt_u32 vcc, 27, v9
v_subrev_u32 v11, 28, v9
v_add_u32 v8, 2, v8
v_cndmask_b32 v9, v9, v11, vcc
v_addc_co_u32 v8, vcc, 0, v8, vcc
v_mul_u32_u24 v23, 0xe00, v8
v_lshl_add_u32 v23, v9, 4, v23
v_subrev_u32 v23, 3072, v23
v_add_u32 v9, 8, v9
v_cmp_lt_u32 vcc, 27, v9
v_subrev_u32 v11, 28, v9
v_add_u32 v8, 2, v8
v_cndmask_b32 v9, v9, v11, vcc
v_addc_co_u32 v8, vcc, 0, v8, vcc
v_mul_u32_u24 v24, 0xe00, v8
v_lshl_add_u32 v24, v9, 4, v24
v_add_u32 v9, 8, v9
v_cmp_lt_u32 vcc, 27, v9
v_subrev_u32 v11, 28, v9
v_add_u32 v8, 2, v8
v_cndmask_b32 v9, v9, v11, vcc
v_addc_co_u32 v8, vcc, 0, v8, vcc
v_mul_u32_u24 v25, 0xe00, v8
v_lshl_add_u32 v25, v9, 4, v25
v_subrev_u32 v25, 1024, v25
v_add_u32 v9, 8, v9
v_cmp_lt_u32 vcc, 27, v9
v_subrev_u32 v11, 28, v9
v_add_u32 v8, 2, v8
v_cndmask_b32 v9, v9, v11, vcc
v_addc_co_u32 v8, vcc, 0, v8, vcc
v_mul_u32_u24 v26, 0xe00, v8
v_lshl_add_u32 v26, v9, 4, v26
v_subrev_u32 v26, 2048, v26
s_mov_b32 m0, 105472
s_waitcnt lgkmcnt(0)
s_load_dwordx2 s[32:33], s[16:17], 0x0
s_add_u32 s18, s18, s34
s_addc_u32 s19, s19, 0
global_load_lds_dwordx4 v20, s[18:19] nt
global_load_lds_dwordx4 v21, s[18:19] offset:1024 nt
global_load_lds_dwordx4 v22, s[18:19] offset:2048 nt
global_load_lds_dwordx4 v23, s[18:19] offset:3072 nt
s_mov_b32 m0, 109568
s_nop 0
global_load_lds_dwordx4 v24, s[18:19] nt
global_load_lds_dwordx4 v25, s[18:19] offset:1024 nt
global_load_lds_dwordx4 v26, s[18:19] offset:2048 nt
v_lshlrev_b32 v10, 4, v1
global_load_dwordx4 v[36:39], v10, s[8:9]
global_load_dwordx4 v[40:43], v10, s[20:21]
s_lshl_b32 s34, s5, 5
v_lshl_add_u32 v10, v2, 3, s34
global_load_dwordx2 v[44:45], v10, s[8:9]
v_lshlrev_b32 v13, 2, v3
v_or_b32 v13, v13, v2
v_mul_u32_u24 v14, 37, v13
v_lshrrev_b32 v14, 8, v14
v_mul_u32_u24 v15, 7, v14
v_sub_u32 v15, v13, v15
v_cmp_lt_u32 vcc, 55, v13
v_subrev_u32 v13, 56, v13
v_mov_b32 v16, 7
v_cndmask_b32 v14, v14, v13, vcc
v_cndmask_b32 v15, v15, v16, vcc
s_lshl_b32 s34, s4, 15
s_lshl_b32 s35, s5, 7
s_add_u32 s34, s34, s35
s_add_u32 s34, s34, 626688
v_lshlrev_b32 v14, 12, v14
v_lshl_or_b32 v14, v15, 4, v14
v_add_u32 v14, s34, v14
v_lshrrev_b32 v19, 2, v3
v_lshl_or_b32 v19, v2, 2, v19
v_mul_u32_u24 v19, 0x1c0, v19
v_and_b32 v20, 3, v1
v_lshl_add_u32 v19, v20, 3, v19
v_add_u32 v19, 105472, v19
v_mov_b32 v30, 118400
v_mov_b32 v7, 0x3ff
s_waitcnt lgkmcnt(0)
s_add_u32 s32, s32, 1
s_addc_u32 s33, s33, 0
s_and_b32 s48, s32, 0x3ff
s_lshr_b32 s49, s32, 10
s_lshr_b32 s50, s32, 20
s_lshr_b64 s[40:41], s[32:33], 30
s_xor_b32 s49, s49, s32
s_xor_b32 s50, s50, s32
s_xor_b32 s51, s40, s32
s_and_b32 s49, s49, 0x3ff
s_and_b32 s50, s50, 0x3ff
s_and_b32 s51, s51, 0x3ff
v_mov_b32 v64, s32
v_mov_b32 v65, s33
s_waitcnt vmcnt(3)
ds_read_b64_tr_b16 v[68:69], v19 offset:0
ds_read_b64_tr_b16 v[70:71], v19 offset:32
ds_read_b64_tr_b16 v[72:73], v19 offset:64
ds_read_b64_tr_b16 v[74:75], v19 offset:96
ds_read_b64_tr_b16 v[76:77], v19 offset:128
ds_read_b64_tr_b16 v[78:79], v19 offset:160
ds_read_b64_tr_b16 v[80:81], v19 offset:192
ds_read_b64_tr_b16 v[82:83], v19 offset:224
ds_read_b64_tr_b16 v[84:85], v19 offset:256
ds_read_b64_tr_b16 v[86:87], v19 offset:288
ds_read_b64_tr_b16 v[88:89], v19 offset:320
ds_read_b64_tr_b16 v[90:91], v19 offset:352
ds_read_b64_tr_b16 v[92:93], v19 offset:384
ds_read_b64_tr_b16 v[94:95], v19 offset:416
s_waitcnt vmcnt(0)
v_dot2_f32_bf16 v50, v36, v40, 0
v_dot2_f32_bf16 v50, v37, v41, v50
v_dot2_f32_bf16 v50, v38, v42, v50
v_dot2_f32_bf16 v50, v39, v43, v50
v_cmp_eq_u32 vcc, 0, v3
s_waitcnt lgkmcnt(0)
s_nop 0
v_cndmask_b32 v100, 0, v44, vcc
v_cndmask_b32 v101, 0, v45, vcc
v_cmp_eq_u32 vcc, 1, v3
s_nop 0
v_mfma_f32_16x16x16_bf16 v[32:35], v[68:69], v[100:101], 0
v_cndmask_b32 v102, 0, v44, vcc
v_cndmask_b32 v103, 0, v45, vcc
v_cmp_eq_u32 vcc, 2, v3
s_nop 0
v_mfma_f32_16x16x16_bf16 v[32:35], v[70:71], v[102:103], v[32:35]
v_cndmask_b32 v100, 0, v44, vcc
v_cndmask_b32 v101, 0, v45, vcc
v_cmp_eq_u32 vcc, 3, v3
s_nop 0
v_mfma_f32_16x16x16_bf16 v[32:35], v[72:73], v[100:101], v[32:35]
v_cndmask_b32 v102, 0, v44, vcc
v_cndmask_b32 v103, 0, v45, vcc
v_cmp_eq_u32 vcc, 4, v3
s_nop 0
v_mfma_f32_16x16x16_bf16 v[32:35], v[74:75], v[102:103], v[32:35]
v_cndmask_b32 v100, 0, v44, vcc
v_cndmask_b32 v101, 0, v45, vcc
v_cmp_eq_u32 vcc, 5, v3
s_nop 0
v_mfma_f32_16x16x16_bf16 v[32:35], v[76:77], v[100:101], v[32:35]
v_cndmask_b32 v102, 0, v44, vcc
v_cndmask_b32 v103, 0, v45, vcc
v_cmp_eq_u32 vcc, 6, v3
s_nop 0
v_mfma_f32_16x16x16_bf16 v[32:35], v[78:79], v[102:103], v[32:35]
v_cndmask_b32 v100, 0, v44, vcc
v_cndmask_b32 v101, 0, v45, vcc
v_cmp_eq_u32 vcc, 7, v3
s_nop 0
v_mfma_f32_16x16x16_bf16 v[32:35], v[80:81], v[100:101], v[32:35]
v_cndmask_b32 v102, 0, v44, vcc
v_cndmask_b32 v103, 0, v45, vcc
v_cmp_eq_u32 vcc, 8, v3
s_nop 0
v_mfma_f32_16x16x16_bf16 v[32:35], v[82:83], v[102:103], v[32:35]
v_cndmask_b32 v100, 0, v44, vcc
v_cndmask_b32 v101, 0, v45, vcc
v_cmp_eq_u32 vcc, 9, v3
s_nop 0
v_mfma_f32_16x16x16_bf16 v[32:35], v[84:85], v[100:101], v[32:35]
v_cndmask_b32 v102, 0, v44, vcc
v_cndmask_b32 v103, 0, v45, vcc
v_cmp_eq_u32 vcc, 10, v3
s_nop 0
v_mfma_f32_16x16x16_bf16 v[32:35], v[86:87], v[102:103], v[32:35]
v_cndmask_b32 v100, 0, v44, vcc
v_cndmask_b32 v101, 0, v45, vcc
v_cmp_eq_u32 vcc, 11, v3
s_nop 0
v_mfma_f32_16x16x16_bf16 v[32:35], v[88:89], v[100:101], v[32:35]
v_cndmask_b32 v102, 0, v44, vcc
v_cndmask_b32 v103, 0, v45, vcc
v_cmp_eq_u32 vcc, 12, v3
s_nop 0
v_mfma_f32_16x16x16_bf16 v[32:35], v[90:91], v[102:103], v[32:35]
v_cndmask_b32 v100, 0, v44, vcc
v_cndmask_b32 v101, 0, v45, vcc
v_cmp_eq_u32 vcc, 13, v3
s_nop 0
v_mfma_f32_16x16x16_bf16 v[32:35], v[92:93], v[100:101], v[32:35]
v_cndmask_b32 v102, 0, v44, vcc
v_cndmask_b32 v103, 0, v45, vcc
v_cmp_eq_u32 vcc, 14, v3
s_nop 0
v_mfma_f32_16x16x16_bf16 v[32:35], v[94:95], v[102:103], v[32:35]
v_add_f32_dpp v50, v50, v50 row_ror:8 row_mask:0xf bank_mask:0xf
s_nop 1
v_add_f32_dpp v50, v50, v50 row_ror:4 row_mask:0xf bank_mask:0xf
s_nop 1
v_add_f32_dpp v50, v50, v50 row_ror:2 row_mask:0xf bank_mask:0xf
s_nop 1
v_add_f32_dpp v50, v50, v50 row_ror:1 row_mask:0xf bank_mask:0xf
v_bfi_b32 v56, v7, s48, v32
v_bfi_b32 v57, v7, s49, v33
v_bfi_b32 v58, v7, s50, v34
)ASM"
R"ASM(v_bfi_b32 v59, v7, s51, v35
global_store_dwordx4 v14, v[56:59], s[16:17] sc1
v_mov_b32 v51, v50
s_nop 1
v_permlane16_swap_b32 v50, v51
v_add_f32 v50, v50, v51
v_mov_b32 v51, v50
s_nop 1
v_permlane32_swap_b32 v50, v51
v_add_f32 v50, v50, v51
v_mul_f32 v50, 0xbfb8aa3b, v50
v_exp_f32 v50, v50
s_nop 0
v_add_f32 v50, 1.0, v50
v_rcp_f32 v50, v50
s_nop 0
ds_write_b32 v30, v50
v_sub_f32 v9, 1.0, v50
s_lshr_b32 s6, s5, 2
s_and_b32 s7, s5, 3
s_lshl_b32 s42, s4, 3
s_add_u32 s42, s42, s6
s_lshl_b32 s35, s42, 13
s_lshl_b32 s36, s7, 11
s_add_u32 s35, s35, s36
s_add_u32 s35, s35, 69632
v_lshl_add_u32 v18, v1, 4, s35
s_lshr_b32 s34, s4, 2
s_lshl_b32 s34, s34, 1
s_bfe_u32 s35, s5, 0x10003
s_add_u32 s34, s34, s35
s_and_b32 s35, s4, 3
s_lshl_b32 s35, s35, 3
s_and_b32 s36, s5, 7
s_add_u32 s35, s35, s36
s_lshl_b32 s36, s34, 16
s_lshr_b32 s37, s35, 2
s_lshl_b32 s37, s37, 7
s_add_u32 s36, s36, s37
s_and_b32 s37, s35, 3
s_lshl_b32 s37, s37, 4
s_add_u32 s36, s36, s37
s_add_u32 s36, s36, 888832
v_lshlrev_b32 v14, 10, v1
v_add_u32 v14, s36, v14
s_lshl_b32 s34, s34, 8
s_lshl_b32 s35, s35, 3
s_add_u32 s34, s34, s35
s_add_u32 s28, s28, s34
s_addc_u32 s29, s29, 0
s_add_u32 s30, s30, s34
s_addc_u32 s31, s31, 0
s_load_dwordx2 s[36:37], s[28:29], 0x0
v_and_b32 v101, 7, v1
v_lshl_or_b32 v101, v101, 2, v2
v_cmp_gt_u32 s[56:57], 28, v101
v_min_u32 v100, 27, v101
s_mul_i32 s34, s42, 56
v_lshl_add_u32 v100, v100, 1, s34
global_load_ushort v102, v100, s[22:23]
v_lshlrev_b32 v101, 1, v101
v_add_u32 v101, 118336, v101
s_lshl_b32 s34, s42, 12
s_add_u32 s34, s34, 626688
v_lshlrev_b32 v100, 4, v1
v_add_u32 v100, s34, v100
s_mov_b32 m0, 118592
v_lshlrev_b32 v15, 1, v2
global_load_lds_dwordx4 v18, s[16:17] sc1
global_load_lds_dwordx4 v18, s[16:17] offset:1024 sc1
s_and_b32 s34, s5, 19
s_cbranch_scc1 .Lgm_nopp
global_load_lds_dwordx4 v14, s[16:17] sc1
.Lgm_nopp:
v_cmp_eq_u32 s[46:47], 0, v3
v_cmp_lt_u32 s[52:53], 1, v2
v_and_b32 v16, 1, v2
v_cmp_eq_u32 s[54:55], 1, v16
s_waitcnt lgkmcnt(0)
v_mov_b32 v16, s36
v_mov_b32 v17, s37
v_cndmask_b32 v16, v16, v17, s[52:53]
v_lshlrev_b32 v17, 16, v16
v_and_b32 v16, 0xffff0000, v16
v_cndmask_b32 v16, v17, v16, s[54:55]
v_mul_f32 v16, v16, v9
s_mov_b32 s43, 131072
s_mov_b32 s38, 0x400000
.Lgm_sp4:
ds_read_b64 v[66:67], v30 offset:8
s_waitcnt lgkmcnt(0)
v_cmp_ne_u64 vcc, v[66:67], v[64:65]
s_cbranch_vccz .Lgm_sp4_d
s_sleep 1
s_sub_u32 s38, s38, 1
s_cbranch_scc0 .Lgm_sp4
.Lgm_sp4_d:
.Lgm_hpoll:
global_load_dwordx4 v[104:107], v100, s[16:17] sc1
global_load_dwordx4 v[108:111], v100, s[16:17] offset:1024 sc1
global_load_dwordx4 v[112:115], v100, s[16:17] offset:2048 sc1
global_load_dwordx4 v[116:119], v100, s[16:17] offset:3072 sc1
s_waitcnt vmcnt(0)
v_xor_b32 v104, s48, v104
v_xor_b32 v105, s49, v105
v_xor_b32 v106, s50, v106
v_xor_b32 v107, s51, v107
v_xor_b32 v108, s48, v108
v_xor_b32 v109, s49, v109
v_xor_b32 v110, s50, v110
v_xor_b32 v111, s51, v111
v_xor_b32 v112, s48, v112
v_xor_b32 v113, s49, v113
v_xor_b32 v114, s50, v114
v_xor_b32 v115, s51, v115
v_xor_b32 v116, s48, v116
v_xor_b32 v117, s49, v117
v_xor_b32 v118, s50, v118
v_xor_b32 v119, s51, v119
v_or3_b32 v40, v104, v105, v106
v_or3_b32 v40, v40, v107, v108
v_or3_b32 v40, v40, v109, v110
v_or3_b32 v40, v40, v111, v112
v_or3_b32 v40, v40, v113, v114
v_or3_b32 v40, v40, v115, v116
v_or3_b32 v40, v40, v117, v118
v_or_b32 v40, v40, v119
v_and_b32 v40, v7, v40
v_cmp_ne_u32 vcc, 0, v40
s_cbranch_vccnz .Lgm_hretry
.Lgm_hok:
v_add_f32 v104, v104, v108
v_add_f32 v112, v112, v116
v_add_f32 v105, v105, v109
v_add_f32 v113, v113, v117
v_add_f32 v106, v106, v110
v_add_f32 v114, v114, v118
v_add_f32 v107, v107, v111
v_add_f32 v115, v115, v119
v_add_f32 v104, v104, v112
v_add_f32 v106, v106, v114
v_add_f32 v105, v105, v113
v_add_f32 v107, v107, v115
v_lshlrev_b32 v102, 16, v102
v_permlane32_swap_b32 v104, v106
v_permlane32_swap_b32 v105, v107
v_add_f32 v104, v104, v106
v_add_f32 v105, v105, v107
s_nop 1
v_permlane16_swap_b32 v104, v105
v_add_f32 v104, v104, v105
s_nop 1
v_add_f32_dpp v104, v104, v104 row_ror:8 row_mask:0xf bank_mask:0xf
v_add_f32 v104, v104, v102
v_mul_f32 v106, v104, v104
v_mov_b32 v107, 0xc0135761
v_fmac_f32 v107, 0xbdd2d3e8, v106
v_mul_f32 v106, v107, v104
v_exp_f32 v106, v106
v_mul_f32 v104, v104, v9
v_add_f32 v106, 1.0, v106
v_rcp_f32 v106, v106
s_nop 0
v_mul_f32 v104, v104, v106
v_cvt_pk_bf16_f32 v104, v104, v104
v_cndmask_b32 v104, 0, v104, s[56:57]
ds_write_b16 v101, v104
s_mov_b32 s43, 131072
s_waitcnt lgkmcnt(0)
s_cmp_ge_u32 s2, 128
s_cbranch_scc1 .Lgm_end
s_barrier
s_barrier
s_sleep 14
.Lgm_fpoll:
global_load_dwordx4 v[32:35], v14, s[16:17] sc1
s_waitcnt vmcnt(0)
v_xor_b32 v32, s48, v32
v_xor_b32 v33, s49, v33
v_xor_b32 v34, s50, v34
v_xor_b32 v35, s51, v35
v_or3_b32 v40, v32, v33, v34
v_or_b32 v40, v40, v35
v_and_b32 v40, v7, v40
v_cmp_ne_u32 vcc, 0, v40
s_cbranch_vccnz .Lgm_fretry
.Lgm_fok:
v_permlane32_swap_b32 v32, v34
v_permlane32_swap_b32 v33, v35
v_add_f32 v32, v32, v34
v_add_f32 v33, v33, v35
s_nop 1
v_permlane16_swap_b32 v32, v33
v_add_f32 v32, v32, v33
s_nop 1
v_add_f32_dpp v32, v32, v32 row_ror:8 row_mask:0xf bank_mask:0xf
s_nop 1
v_add_f32_dpp v32, v32, v32 row_ror:4 row_mask:0xf bank_mask:0xf
s_nop 1
v_add_f32_dpp v32, v32, v32 row_ror:2 row_mask:0xf bank_mask:0xf
s_nop 1
v_add_f32_dpp v32, v32, v32 row_ror:1 row_mask:0xf bank_mask:0xf
v_add_f32 v32, v32, v16
v_cvt_pk_bf16_f32 v32, v32, v32
s_mov_b64 exec, s[46:47]
global_store_short v15, v32, s[30:31]
s_cmp_lg_u32 s2, 0
s_cbranch_scc1 .Lgm_fin
s_mov_b64 exec, 1
v_mov_b32 v34, s32
v_mov_b32 v35, s33
v_mov_b32 v33, 0
global_store_dwordx2 v33, v[34:35], s[16:17] sc1
.Lgm_fin:
s_endpgm
.Lgm_hretry:
s_sleep 4
s_sub_u32 s43, s43, 1
s_cbranch_scc0 .Lgm_hpoll
s_branch .Lgm_hok
.Lgm_fretry:
s_sleep 2
s_sub_u32 s43, s43, 1
s_cbranch_scc0 .Lgm_fpoll
s_branch .Lgm_fok
.Lgm_end:
s_endpgm
)ASM"
      :
      : "{s[0:1]}"(__builtin_amdgcn_kernarg_segment_ptr()), "{s2}"(blockIdx.x), "{v0}"(threadIdx.x), "s"((unsigned)(size_t)(__attribute__((address_space(3))) char*)smem)
      : "v1", "v2", "v3", "v4", "v5", "v6", "v7", "v8", "v9", "v10", "v11", "v12", "v13", "v14", "v15", "v16", "v17", "v18", "v19", "v20", "v21", "v22", "v23", "v24", "v25", "v26", "v27", "v28", "v29", "v30", "v31", "v32", "v33", "v34", "v35", "v36", "v37", "v38", "v39", "v40", "v41", "v42", "v43", "v44", "v45", "v46", "v47", "v48", "v49", "v50", "v51", "v52", "v53", "v54", "v55", "v56", "v57", "v58", "v59", "v60", "v61", "v62", "v63", "v64", "v65", "v66", "v67", "v68", "v69", "v70", "v71", "v72", "v73", "v74", "v75", "v76", "v77", "v78", "v79", "v80", "v81", "v82", "v83", "v84", "v85", "v86", "v87", "v88", "v89", "v90", "v91", "v92", "v93", "v94", "v95", "v96", "v97", "v98", "v99", "v100", "v101", "v102", "v103", "v104", "v105", "v106", "v107", "v108", "v109", "v110", "v111", "v112", "v113", "v114", "v115", "v116", "v117", "v118", "v119", "s3", "s4", "s5", "s6", "s7", "s8", "s9", "s10", "s11", "s12", "s13", "s14", "s15", "s16", "s17", "s18", "s19", "s20", "s21", "s22", "s23", "s24", "s25", "s26", "s27", "s28", "s29", "s30", "s31", "s33", "s34", "s35", "s36", "s37", "s38", "s39", "s40", "s41", "s42", "s43", "s44", "s45", "s46", "s47", "s48", "s49", "s50", "s51", "s52", "s53", "s54", "s55", "s56", "s57", "vcc", "memory");
}

extern "C" void kernel_launch(void* const* d_in, const int* in_sizes, int n_in, void* d_out, int out_size, void* d_ws, size_t ws_size, hipStream_t stream) {
  gated_mix_kernel<<<256, 640, 0, stream>>>((const u16*)d_in[0], (const u16*)d_in[3], (const u16*)d_in[1], (const u16*)d_in[2], (char*)d_ws, (const u16*)d_in[6],
                                            (const u16*)d_in[10], (const u16*)d_in[7], (const u16*)d_in[8], (const u16*)d_in[4], (const u16*)d_in[9], (u16*)d_out);
}
```
